```python
import torch

def _softmax(x, axis=-1):
    return torch.softmax(x, dim=axis)
def _gelu(x):
    return torch.nn.functional.gelu(x, approximate="tanh")
def _silu(x):
    return torch.nn.functional.silu(x)
def _sigmoid(x):
    return torch.sigmoid(x)
def _layernorm(x, axis=-1, eps=1e-5):
    m = x.mean(dim=axis, keepdim=True)
    v = x.var(dim=axis, keepdim=True, unbiased=False)
    return (x - m) / torch.sqrt(v + eps)
def _scatter_add(tgt, idx, src, axis=0):
    return tgt.clone().index_add_(axis, idx, src)
def _segment_sum(src, seg, n):
    out = torch.zeros((n,) + src.shape[1:], dtype=src.dtype)
    return out.index_add_(0, seg, src)
def _pad(x, pw):
    flat = tuple(v for pair in reversed(pw) for v in pair)
    return torch.nn.functional.pad(x, flat)

kernel_name = 'interleave_layers__mha_block__glu_gated_bilinear__prompt'

def setup_inputs():

    _w = (lambda a: (lambda t: t.to(torch.bfloat16) if t.is_floating_point() else t)(torch.as_tensor(a)))
    def _A_setup():
        B, S, D, H = 1, 2048, 512, 32
        return {
            "x": _w((lambda *s: torch.randn(*s, dtype=torch.bfloat16) * 0.05)(B, S, D)),
            "wqkv": _w((lambda *s: torch.randn(*s, dtype=torch.bfloat16) * 0.05)(D, 3 * D)),
            "wo": _w((lambda *s: torch.randn(*s, dtype=torch.bfloat16) * 0.05)(D, D)),
        }
    _A_in = _A_setup()

    def _B_setup():
        B, S, D, F = 1, 2048, 512, 1792
        return {
            "x": _w((lambda *s: torch.randn(*s, dtype=torch.bfloat16) * 0.05)(B, S, D)),
            "w_gate": _w((lambda *s: torch.randn(*s, dtype=torch.bfloat16) * 0.4)(D, F) * (D ** -0.5)),
            "w_up": _w((lambda *s: torch.randn(*s, dtype=torch.bfloat16) * 0.4)(D, F) * (D ** -0.5)),
            "w_down": _w((lambda *s: torch.randn(*s, dtype=torch.bfloat16) * 0.4)(F, D) * (F ** -0.5)),
        }
    _B_in = _B_setup()

    return {
        "A_x": _A_in["x"],
        "A_wqkv": _A_in["wqkv"],
        "A_wo": _A_in["wo"],
        "B_x": _B_in["x"],
        "B_w_gate": _B_in["w_gate"],
        "B_w_up": _B_in["w_up"],
        "B_w_down": _B_in["w_down"],
    }

def reference(A_x, A_wqkv, A_wo, B_x, B_w_gate, B_w_up, B_w_down):
    def _A_ref(x, wqkv, wo):
        B, S, D = x.shape
        H = 32
        Dh = D // H
        qkv = (lambda x, *d: x.permute(*d))((x @ wqkv).reshape(B, S, 3, H, Dh), 2, 0, 3, 1, 4)
        q, k, v = qkv[0], qkv[1], qkv[2]
        scores = torch.einsum("bhqd,bhkd->bhqk", q, k) * (Dh ** -0.5)
        p = _softmax(scores, axis=-1)
        o = torch.einsum("bhqk,bhkd->bhqd", p, v)
        return (lambda x, *d: x.permute(*d))(o, 0, 2, 1, 3).reshape(B, S, D) @ wo

    def _B_ref(x, w_gate, w_up, w_down):
        g = torch.einsum("bsd,df->bsf", x, w_gate)
        u = torch.einsum("bsd,df->bsf", x, w_up)
        h = g * u
        return torch.einsum("bsf,fd->bsd", h, w_down)

    x = A_x
    x = x + _A_ref(_layernorm(x, axis=-1), A_wqkv, A_wo)
    x = x + _B_ref(_layernorm(x, axis=-1), B_w_gate, B_w_up, B_w_down)
    x = x + _A_ref(_layernorm(x, axis=-1), A_wqkv, A_wo)
    x = x + _B_ref(_layernorm(x, axis=-1), B_w_gate, B_w_up, B_w_down)
    return x
```

```cpp
#include <hip/hip_runtime.h>
#include <hip/hip_bf16.h>
#include <stdint.h>

#define GAS __attribute__((address_space(1)))
#define LAS __attribute__((address_space(3)))
#if defined(__HIP_DEVICE_COMPILE__)
#define NOPK __attribute__((target("no-packed-fp32-ops")))
#else
#define NOPK
#endif
#define TID ((int)__builtin_amdgcn_workitem_id_x())
#define BID ((int)__builtin_amdgcn_workgroup_id_x())
#define DI __device__ __forceinline__ NOPK
#define RLX_AGENT __ATOMIC_RELAXED, __HIP_MEMORY_SCOPE_AGENT
typedef unsigned v4u __attribute__((ext_vector_type(4)));
typedef unsigned v2u __attribute__((ext_vector_type(2)));
typedef float f32x2 __attribute__((ext_vector_type(2)));
typedef float f32x4 __attribute__((ext_vector_type(4)));
typedef float f32x8 __attribute__((ext_vector_type(8)));
typedef float f32x16 __attribute__((ext_vector_type(16)));
typedef __bf16 bf16x2 __attribute__((ext_vector_type(2)));
typedef __bf16 bf16x8 __attribute__((ext_vector_type(8)));
typedef __bf16 bf16x16 __attribute__((ext_vector_type(16)));
typedef __amdgpu_buffer_rsrc_t rsrc_t;
typedef LAS unsigned char* lptr;

#define ATTN_INIT_V \
  "s_nop 2\n" \
  "v_mov_b32 v110, %[one]\n" \
  "v_mov_b32 v111, %[one]\n" \
  "v_mov_b32 v112, %[one]\n" \
  "v_mov_b32 v113, %[one]\n" \
  "v_mov_b32 v114, %[one]\n" \
  "v_mov_b32 v115, %[one]\n" \
  "v_mov_b32 v116, %[one]\n" \
  "v_mov_b32 v117, %[one]\n" \
  "v_mov_b32 v118, %[one]\n" \
  "v_mov_b32 v119, %[one]\n" \
  "v_mov_b32 v120, %[one]\n" \
  "v_mov_b32 v121, %[one]\n" \
  "v_mov_b32 v122, %[one]\n" \
  "v_mov_b32 v123, %[one]\n" \
  "v_mov_b32 v124, %[one]\n" \
  "v_mov_b32 v125, %[one]\n" \
  ""
#define ATTN_INIT_A \
  "s_nop 2\n" \
  "v_mov_b32 v56, 0\n" \
  "v_mov_b32 v57, 0\n" \
  "v_mov_b32 v58, 0\n" \
  "v_mov_b32 v59, 0\n" \
  "v_mov_b32 v60, 0\n" \
  "v_mov_b32 v61, 0\n" \
  "v_mov_b32 v62, 0\n" \
  "v_mov_b32 v63, 0\n" \
  "v_mov_b32 v64, 0\n" \
  "v_mov_b32 v66, 0\n" \
  "v_mov_b32 v67, 0\n" \
  "v_mov_b32 v68, 0\n" \
  "v_mov_b32 v69, 0\n" \
  "v_mov_b32 v70, 0\n" \
  "v_mov_b32 v71, 0\n" \
  "v_mov_b32 v72, 0\n" \
  "v_mov_b32 v73, 0\n" \
  "v_mov_b32 v74, 0\n" \
  ""
#define ATTN_INIT_B \
  "s_nop 2\n" \
  "v_mov_b32 v76, 0\n" \
  "v_mov_b32 v77, 0\n" \
  "v_mov_b32 v78, 0\n" \
  "v_mov_b32 v79, 0\n" \
  "v_mov_b32 v80, 0\n" \
  "v_mov_b32 v81, 0\n" \
  "v_mov_b32 v82, 0\n" \
  "v_mov_b32 v83, 0\n" \
  "v_mov_b32 v84, 0\n" \
  "v_mov_b32 v86, 0\n" \
  "v_mov_b32 v87, 0\n" \
  "v_mov_b32 v88, 0\n" \
  "v_mov_b32 v89, 0\n" \
  "v_mov_b32 v90, 0\n" \
  "v_mov_b32 v91, 0\n" \
  "v_mov_b32 v92, 0\n" \
  "v_mov_b32 v93, 0\n" \
  "v_mov_b32 v94, 0\n" \
  ""
#define ATTN_OWN_ASM \
  "ds_read_b128 v[102:105], %[lk]\n" \
  "s_mov_b64 exec, %[vm]\n" \
  "ds_read_b128 v[110:113], %[lv]\n" \
  "ds_read_b128 v[114:117], %[lv] offset:512\n" \
  "s_mov_b64 exec, -1\n" \
  "v_mov_b32 v6, 0xdddd\n" \
  "s_waitcnt lgkmcnt(2)\n" \
  "v_mfma_f32_32x32x16_bf16 v[24:39], v[102:105], v[8:11], 0\n" \
  "v_mov_b32 v48, 0\n" \
  "v_mov_b32 v49, 0\n" \
  "v_mov_b32 v50, 0\n" \
  "v_mov_b32 v51, 0\n" \
  "v_mov_b32 v52, 0\n" \
  "v_mov_b32 v53, 0\n" \
  "v_mov_b32 v54, 0\n" \
  "v_mov_b32 v55, 0\n" \
  "s_nop 2\n" \
  "s_waitcnt lgkmcnt(0)\n" \
  "v_exp_f32 v24, v24\n" \
  "v_exp_f32 v25, v25\n" \
  "v_smfmac_f32_32x32x32_bf16 v[86:101], v[122:125], v[48:55], v6\n" \
  "v_exp_f32 v26, v26\n" \
  "v_exp_f32 v27, v27\n" \
  "v_exp_f32 v28, v28\n" \
  "v_mfma_f32_32x32x16_bf16 v[40:55], v[102:105], v[12:15], 0\n" \
  "v_exp_f32 v29, v29\n" \
  "v_exp_f32 v30, v30\n" \
  "v_exp_f32 v31, v31\n" \
  "v_exp_f32 v32, v32\n" \
  "v_exp_f32 v33, v33\n" \
  "v_exp_f32 v34, v34\n" \
  "v_smfmac_f32_32x32x32_bf16 v[56:71], v[110:113], v[24:31], v6\n" \
  "v_exp_f32 v35, v35\n" \
  "v_exp_f32 v36, v36\n" \
  "v_exp_f32 v37, v37\n" \
  "v_exp_f32 v38, v38\n" \
  "v_exp_f32 v39, v39\n" \
  "v_exp_f32 v40, v40\n" \
  "v_exp_f32 v41, v41\n" \
  "v_smfmac_f32_32x32x32_bf16 v[56:71], v[114:117], v[32:39], v6\n" \
  "v_exp_f32 v42, v42\n" \
  "v_exp_f32 v43, v43\n" \
  "v_exp_f32 v44, v44\n" \
  "v_mfma_f32_32x32x16_bf16 v[24:39], v[102:105], v[16:19], 0\n" \
  "v_exp_f32 v45, v45\n" \
  "v_exp_f32 v46, v46\n" \
  "v_exp_f32 v47, v47\n" \
  "v_exp_f32 v48, v48\n" \
  "v_exp_f32 v49, v49\n" \
  "v_exp_f32 v50, v50\n" \
  "v_smfmac_f32_32x32x32_bf16 v[66:81], v[110:113], v[40:47], v6\n" \
  "v_exp_f32 v51, v51\n" \
  "v_exp_f32 v52, v52\n" \
  "v_exp_f32 v53, v53\n" \
  "v_exp_f32 v54, v54\n" \
  "v_exp_f32 v55, v55\n" \
  "v_exp_f32 v24, v24\n" \
  "v_exp_f32 v25, v25\n" \
  "v_smfmac_f32_32x32x32_bf16 v[66:81], v[114:117], v[48:55], v6\n" \
  "v_exp_f32 v26, v26\n" \
  "v_exp_f32 v27, v27\n" \
  "v_exp_f32 v28, v28\n" \
  "v_mfma_f32_32x32x16_bf16 v[40:55], v[102:105], v[20:23], 0\n" \
  "v_exp_f32 v29, v29\n" \
  "v_exp_f32 v30, v30\n" \
  "v_exp_f32 v31, v31\n" \
  "v_exp_f32 v32, v32\n" \
  "v_exp_f32 v33, v33\n" \
  "v_exp_f32 v34, v34\n" \
  "v_smfmac_f32_32x32x32_bf16 v[76:91], v[110:113], v[24:31], v6\n" \
  "v_exp_f32 v35, v35\n" \
  "v_exp_f32 v36, v36\n" \
  "v_exp_f32 v37, v37\n" \
  "v_exp_f32 v38, v38\n" \
  "v_exp_f32 v39, v39\n" \
  "v_exp_f32 v40, v40\n" \
  "v_exp_f32 v41, v41\n" \
  "v_smfmac_f32_32x32x32_bf16 v[76:91], v[114:117], v[32:39], v6\n" \
  "v_exp_f32 v42, v42\n" \
  "v_exp_f32 v43, v43\n" \
  "v_exp_f32 v44, v44\n" \
  "s_nop 0\n" \
  "v_exp_f32 v45, v45\n" \
  "v_exp_f32 v46, v46\n" \
  "v_exp_f32 v47, v47\n" \
  "v_exp_f32 v48, v48\n" \
  "v_exp_f32 v49, v49\n" \
  "v_exp_f32 v50, v50\n" \
  "v_smfmac_f32_32x32x32_bf16 v[86:101], v[110:113], v[40:47], v6\n" \
  "v_exp_f32 v51, v51\n" \
  "v_exp_f32 v52, v52\n" \
  "v_exp_f32 v53, v53\n" \
  "v_exp_f32 v54, v54\n" \
  "v_exp_f32 v55, v55\n" \
  "s_nop 1\n" \
  "v_smfmac_f32_32x32x32_bf16 v[86:101], v[114:117], v[48:55], v6\n" \
  "s_nop 11\n" \
  ""
#define ATTN_LOOP_ASM \
  "buffer_load_dwordx4 v[102:105], v4, %[rs], %[so] offen sc1\n" \
  "s_mov_b64 exec, %[vm]\n buffer_load_dwordx4 v[110:113], v5, %[rs], %[so] offen sc1\n buffer_load_dwordx4 v[114:117], v5, %[rs], %[so] offen offset:512 sc1\n s_mov_b64 exec, -1\n" \
  "s_add_u32 %[ti], %[ti], %[st]\n" \
  "s_and_b32 %[ti], %[ti], 0x1ffff\n" \
  "s_add_u32 %[so], %[ti], %[hb]\n" \
  "v_mov_b32 v6, 0xdddd\n" \
  "v_mov_b32 v48, 0\n" \
  "v_mov_b32 v49, 0\n" \
  "v_mov_b32 v50, 0\n" \
  "v_mov_b32 v51, 0\n" \
  "v_mov_b32 v52, 0\n" \
  "v_mov_b32 v53, 0\n" \
  "v_mov_b32 v54, 0\n" \
  "v_mov_b32 v55, 0\n" \
  "v_mov_b32 v142, -1\n" \
  "v_mov_b32 v143, -1\n" \
  "v_mov_b32 v144, -1\n" \
  "v_mov_b32 v145, -1\n" \
  "v_mov_b32 v146, %[tl]\n" \
  "v_mov_b32 v147, %[th]\n" \
  "s_waitcnt vmcnt(0)\n" \
  "v_mfma_f32_32x32x16_bf16 v[24:39], v[102:105], v[8:11], 0\n" \
  "s_nop 9\n" \
  "s_cmp_eq_u32 %[gc], 0\n" \
  "s_cbranch_scc1 1f\n" \
  "v_exp_f32 v24, v24\n" \
  "v_exp_f32 v25, v25\n" \
  "v_smfmac_f32_32x32x32_bf16 v[86:101], v[122:125], v[48:55], v6\n" \
  "v_exp_f32 v26, v26\n" \
  "v_exp_f32 v27, v27\n" \
  "v_exp_f32 v28, v28\n" \
  "v_mfma_f32_32x32x16_bf16 v[40:55], v[102:105], v[12:15], 0\n" \
  "v_exp_f32 v29, v29\n" \
  "buffer_load_dwordx4 v[106:109], v4, %[rs], %[so] offen sc1\n s_mov_b64 exec, %[vm]\n buffer_load_dwordx4 v[118:121], v5, %[rs], %[so] offen sc1\n buffer_load_dwordx4 v[122:125], v5, %[rs], %[so] offen offset:512 sc1\n s_mov_b64 exec, -1\n" \
  "v_exp_f32 v30, v30\n" \
  "s_add_u32 %[ti], %[ti], %[st]\n" \
  "v_exp_f32 v31, v31\n" \
  "s_and_b32 %[ti], %[ti], 0x1ffff\n" \
  "v_exp_f32 v32, v32\n" \
  "s_add_u32 %[so], %[ti], %[hb]\n" \
  "v_exp_f32 v33, v33\n" \
  "v_exp_f32 v34, v34\n" \
  "v_smfmac_f32_32x32x32_bf16 v[56:71], v[110:113], v[24:31], v6\n" \
  "v_exp_f32 v35, v35\n" \
  "v_exp_f32 v36, v36\n" \
  "v_exp_f32 v37, v37\n" \
  "v_exp_f32 v38, v38\n" \
  "v_exp_f32 v39, v39\n" \
  "v_exp_f32 v40, v40\n" \
  "v_exp_f32 v41, v41\n" \
  "v_smfmac_f32_32x32x32_bf16 v[56:71], v[114:117], v[32:39], v6\n" \
  "v_exp_f32 v42, v42\n" \
  "v_exp_f32 v43, v43\n" \
  "v_exp_f32 v44, v44\n" \
  "v_mfma_f32_32x32x16_bf16 v[24:39], v[102:105], v[16:19], 0\n" \
  "v_exp_f32 v45, v45\n" \
  "buffer_store_dwordx4 v[126:129], v4, %[rs], %[td0] offen sc1\n" \
  "v_exp_f32 v46, v46\n" \
  "v_exp_f32 v47, v47\n" \
  "v_exp_f32 v48, v48\n" \
  "v_exp_f32 v49, v49\n" \
  "v_exp_f32 v50, v50\n" \
  "v_smfmac_f32_32x32x32_bf16 v[66:81], v[110:113], v[40:47], v6\n" \
  "v_exp_f32 v51, v51\n" \
  "v_exp_f32 v52, v52\n" \
  "v_exp_f32 v53, v53\n" \
  "v_exp_f32 v54, v54\n" \
  "v_exp_f32 v55, v55\n" \
  "v_exp_f32 v24, v24\n" \
  "v_exp_f32 v25, v25\n" \
  "v_smfmac_f32_32x32x32_bf16 v[66:81], v[114:117], v[48:55], v6\n" \
  "v_exp_f32 v26, v26\n" \
  "v_exp_f32 v27, v27\n" \
  "v_exp_f32 v28, v28\n" \
  "v_mfma_f32_32x32x16_bf16 v[40:55], v[102:105], v[20:23], 0\n" \
  "v_exp_f32 v29, v29\n" \
  "buffer_store_dwordx4 v[142:145], v4, %[rs], %[fo] offen sc1\n" \
  "v_exp_f32 v30, v30\n" \
  "s_add_u32 %[fo], %[fo], 0x200000\n" \
  "v_exp_f32 v31, v31\n" \
  "v_exp_f32 v32, v32\n" \
  "v_exp_f32 v33, v33\n" \
  "v_exp_f32 v34, v34\n" \
  "v_smfmac_f32_32x32x32_bf16 v[76:91], v[110:113], v[24:31], v6\n" \
  "v_exp_f32 v35, v35\n" \
  "v_exp_f32 v36, v36\n" \
  "v_exp_f32 v37, v37\n" \
  "v_exp_f32 v38, v38\n" \
  "v_exp_f32 v39, v39\n" \
  "v_exp_f32 v40, v40\n" \
  "v_exp_f32 v41, v41\n" \
  "v_smfmac_f32_32x32x32_bf16 v[76:91], v[114:117], v[32:39], v6\n" \
  "v_exp_f32 v42, v42\n" \
  "v_exp_f32 v43, v43\n" \
  "v_exp_f32 v44, v44\n" \
  "s_waitcnt vmcnt(2)\n" \
  "v_mfma_f32_32x32x16_bf16 v[24:39], v[106:109], v[8:11], 0\n" \
  "v_exp_f32 v45, v45\n" \
  "v_exp_f32 v46, v46\n" \
  "v_exp_f32 v47, v47\n" \
  "v_exp_f32 v48, v48\n" \
  "v_exp_f32 v49, v49\n" \
  "v_exp_f32 v50, v50\n" \
  "v_smfmac_f32_32x32x32_bf16 v[86:101], v[110:113], v[40:47], v6\n" \
  "v_exp_f32 v51, v51\n" \
  "v_exp_f32 v52, v52\n" \
  "v_exp_f32 v53, v53\n" \
  "v_exp_f32 v54, v54\n" \
  "v_exp_f32 v55, v55\n" \
  "v_exp_f32 v24, v24\n" \
  "v_exp_f32 v25, v25\n" \
  "v_smfmac_f32_32x32x32_bf16 v[86:101], v[114:117], v[48:55], v6\n" \
  "v_exp_f32 v26, v26\n" \
  "v_exp_f32 v27, v27\n" \
  "v_exp_f32 v28, v28\n" \
  "v_mfma_f32_32x32x16_bf16 v[40:55], v[106:109], v[12:15], 0\n" \
  "v_exp_f32 v29, v29\n" \
  "buffer_load_dwordx4 v[102:105], v4, %[rs], %[so] offen sc1\n s_mov_b64 exec, %[vm]\n buffer_load_dwordx4 v[110:113], v5, %[rs], %[so] offen sc1\n buffer_load_dwordx4 v[114:117], v5, %[rs], %[so] offen offset:512 sc1\n s_mov_b64 exec, -1\n" \
  "v_exp_f32 v30, v30\n" \
  "s_add_u32 %[ti], %[ti], %[st]\n" \
  "v_exp_f32 v31, v31\n" \
  "s_and_b32 %[ti], %[ti], 0x1ffff\n" \
  "v_exp_f32 v32, v32\n" \
  "s_add_u32 %[so], %[ti], %[hb]\n" \
  "v_exp_f32 v33, v33\n" \
  "v_exp_f32 v34, v34\n" \
  "v_smfmac_f32_32x32x32_bf16 v[56:71], v[118:121], v[24:31], v6\n" \
  "v_exp_f32 v35, v35\n" \
  "v_exp_f32 v36, v36\n" \
  "v_exp_f32 v37, v37\n" \
  "v_exp_f32 v38, v38\n" \
  "v_exp_f32 v39, v39\n" \
  "v_exp_f32 v40, v40\n" \
  "v_exp_f32 v41, v41\n" \
  "v_smfmac_f32_32x32x32_bf16 v[56:71], v[122:125], v[32:39], v6\n" \
  "v_exp_f32 v42, v42\n" \
  "v_exp_f32 v43, v43\n" \
  "v_exp_f32 v44, v44\n" \
  "v_mfma_f32_32x32x16_bf16 v[24:39], v[106:109], v[16:19], 0\n" \
  "v_exp_f32 v45, v45\n" \
  "buffer_store_dwordx4 v[130:133], v4, %[rs], %[td1] offen sc1\n" \
  "v_exp_f32 v46, v46\n" \
  "v_exp_f32 v47, v47\n" \
  "v_exp_f32 v48, v48\n" \
  "v_exp_f32 v49, v49\n" \
  "v_exp_f32 v50, v50\n" \
  "v_smfmac_f32_32x32x32_bf16 v[66:81], v[118:121], v[40:47], v6\n" \
  "v_exp_f32 v51, v51\n" \
  "v_exp_f32 v52, v52\n" \
  "v_exp_f32 v53, v53\n" \
  "v_exp_f32 v54, v54\n" \
  "v_exp_f32 v55, v55\n" \
  "v_exp_f32 v24, v24\n" \
  "v_exp_f32 v25, v25\n" \
  "v_smfmac_f32_32x32x32_bf16 v[66:81], v[122:125], v[48:55], v6\n" \
  "v_exp_f32 v26, v26\n" \
  "v_exp_f32 v27, v27\n" \
  "v_exp_f32 v28, v28\n" \
  "v_mfma_f32_32x32x16_bf16 v[40:55], v[106:109], v[20:23], 0\n" \
  "v_exp_f32 v29, v29\n" \
  "buffer_store_dwordx4 v[142:145], %[vx], %[rs], %[fx] offen sc1\n" \
  "v_exp_f32 v30, v30\n" \
  "s_add_u32 %[fx], %[fx], 0x200000\n" \
  "v_exp_f32 v31, v31\n" \
  "v_exp_f32 v32, v32\n" \
  "v_exp_f32 v33, v33\n" \
  "v_exp_f32 v34, v34\n" \
  "v_smfmac_f32_32x32x32_bf16 v[76:91], v[118:121], v[24:31], v6\n" \
  "v_exp_f32 v35, v35\n" \
  "v_exp_f32 v36, v36\n" \
  "v_exp_f32 v37, v37\n" \
  "v_exp_f32 v38, v38\n" \
  "v_exp_f32 v39, v39\n" \
  "v_exp_f32 v40, v40\n" \
  "v_exp_f32 v41, v41\n" \
  "v_smfmac_f32_32x32x32_bf16 v[76:91], v[122:125], v[32:39], v6\n" \
  "v_exp_f32 v42, v42\n" \
  "v_exp_f32 v43, v43\n" \
  "v_exp_f32 v44, v44\n" \
  "s_waitcnt vmcnt(2)\n" \
  "v_mfma_f32_32x32x16_bf16 v[24:39], v[102:105], v[8:11], 0\n" \
  "v_exp_f32 v45, v45\n" \
  "v_exp_f32 v46, v46\n" \
  "v_exp_f32 v47, v47\n" \
  "v_exp_f32 v48, v48\n" \
  "v_exp_f32 v49, v49\n" \
  "v_exp_f32 v50, v50\n" \
  "v_smfmac_f32_32x32x32_bf16 v[86:101], v[118:121], v[40:47], v6\n" \
  "v_exp_f32 v51, v51\n" \
  "v_exp_f32 v52, v52\n" \
  "v_exp_f32 v53, v53\n" \
  "v_exp_f32 v54, v54\n" \
  "v_exp_f32 v55, v55\n" \
  "s_barrier\n" \
  "v_exp_f32 v24, v24\n" \
  "v_exp_f32 v25, v25\n" \
  "v_smfmac_f32_32x32x32_bf16 v[86:101], v[122:125], v[48:55], v6\n" \
  "v_exp_f32 v26, v26\n" \
  "v_exp_f32 v27, v27\n" \
  "v_exp_f32 v28, v28\n" \
  "v_mfma_f32_32x32x16_bf16 v[40:55], v[102:105], v[12:15], 0\n" \
  "v_exp_f32 v29, v29\n" \
  "buffer_load_dwordx4 v[106:109], v4, %[rs], %[so] offen sc1\n s_mov_b64 exec, %[vm]\n buffer_load_dwordx4 v[118:121], v5, %[rs], %[so] offen sc1\n buffer_load_dwordx4 v[122:125], v5, %[rs], %[so] offen offset:512 sc1\n s_mov_b64 exec, -1\n" \
  "v_exp_f32 v30, v30\n" \
  "s_add_u32 %[ti], %[ti], %[st]\n" \
  "v_exp_f32 v31, v31\n" \
  "s_and_b32 %[ti], %[ti], 0x1ffff\n" \
  "v_exp_f32 v32, v32\n" \
  "s_add_u32 %[so], %[ti], %[hb]\n" \
  "v_exp_f32 v33, v33\n" \
  "v_exp_f32 v34, v34\n" \
  "v_smfmac_f32_32x32x32_bf16 v[56:71], v[110:113], v[24:31], v6\n" \
  "v_exp_f32 v35, v35\n" \
  "v_exp_f32 v36, v36\n" \
  "v_exp_f32 v37, v37\n" \
  "v_exp_f32 v38, v38\n" \
  "v_exp_f32 v39, v39\n" \
  "v_exp_f32 v40, v40\n" \
  "v_exp_f32 v41, v41\n" \
  "v_smfmac_f32_32x32x32_bf16 v[56:71], v[114:117], v[32:39], v6\n" \
  "v_exp_f32 v42, v42\n" \
  "v_exp_f32 v43, v43\n" \
  "v_exp_f32 v44, v44\n" \
  "v_mfma_f32_32x32x16_bf16 v[24:39], v[102:105], v[16:19], 0\n" \
  "v_exp_f32 v45, v45\n" \
  "buffer_store_dwordx4 v[134:137], v4, %[rs], %[td2] offen sc1\n" \
  "v_exp_f32 v46, v46\n" \
  "v_exp_f32 v47, v47\n" \
  "v_exp_f32 v48, v48\n" \
  "v_exp_f32 v49, v49\n" \
  "v_exp_f32 v50, v50\n" \
  "v_smfmac_f32_32x32x32_bf16 v[66:81], v[110:113], v[40:47], v6\n" \
  "v_exp_f32 v51, v51\n" \
  "v_exp_f32 v52, v52\n" \
  "v_exp_f32 v53, v53\n" \
  "v_exp_f32 v54, v54\n" \
  "v_exp_f32 v55, v55\n" \
  "v_exp_f32 v24, v24\n" \
  "v_exp_f32 v25, v25\n" \
  "v_smfmac_f32_32x32x32_bf16 v[66:81], v[114:117], v[48:55], v6\n" \
  "v_exp_f32 v26, v26\n" \
  "v_exp_f32 v27, v27\n" \
  "v_exp_f32 v28, v28\n" \
  "v_mfma_f32_32x32x16_bf16 v[40:55], v[102:105], v[20:23], 0\n" \
  "v_exp_f32 v29, v29\n" \
  "buffer_store_dwordx4 v[142:145], %[vx], %[rs], %[fx] offen sc1\n" \
  "v_exp_f32 v30, v30\n" \
  "s_add_u32 %[fx], %[fx], 0x200000\n" \
  "v_exp_f32 v31, v31\n" \
  "v_exp_f32 v32, v32\n" \
  "v_exp_f32 v33, v33\n" \
  "v_exp_f32 v34, v34\n" \
  "v_smfmac_f32_32x32x32_bf16 v[76:91], v[110:113], v[24:31], v6\n" \
  "v_exp_f32 v35, v35\n" \
  "v_exp_f32 v36, v36\n" \
  "v_exp_f32 v37, v37\n" \
  "v_exp_f32 v38, v38\n" \
  "v_exp_f32 v39, v39\n" \
  "v_exp_f32 v40, v40\n" \
  "v_exp_f32 v41, v41\n" \
  "v_smfmac_f32_32x32x32_bf16 v[76:91], v[114:117], v[32:39], v6\n" \
  "v_exp_f32 v42, v42\n" \
  "v_exp_f32 v43, v43\n" \
  "v_exp_f32 v44, v44\n" \
  "s_waitcnt vmcnt(2)\n" \
  "v_mfma_f32_32x32x16_bf16 v[24:39], v[106:109], v[8:11], 0\n" \
  "v_exp_f32 v45, v45\n" \
  "v_exp_f32 v46, v46\n" \
  "v_exp_f32 v47, v47\n" \
  "v_exp_f32 v48, v48\n" \
  "v_exp_f32 v49, v49\n" \
  "v_exp_f32 v50, v50\n" \
  "v_smfmac_f32_32x32x32_bf16 v[86:101], v[110:113], v[40:47], v6\n" \
  "v_exp_f32 v51, v51\n" \
  "v_exp_f32 v52, v52\n" \
  "v_exp_f32 v53, v53\n" \
  "v_exp_f32 v54, v54\n" \
  "v_exp_f32 v55, v55\n" \
  "v_exp_f32 v24, v24\n" \
  "v_exp_f32 v25, v25\n" \
  "v_smfmac_f32_32x32x32_bf16 v[86:101], v[114:117], v[48:55], v6\n" \
  "v_exp_f32 v26, v26\n" \
  "v_exp_f32 v27, v27\n" \
  "v_exp_f32 v28, v28\n" \
  "v_mfma_f32_32x32x16_bf16 v[40:55], v[106:109], v[12:15], 0\n" \
  "v_exp_f32 v29, v29\n" \
  "buffer_load_dwordx4 v[102:105], v4, %[rs], %[so] offen sc1\n s_mov_b64 exec, %[vm]\n buffer_load_dwordx4 v[110:113], v5, %[rs], %[so] offen sc1\n buffer_load_dwordx4 v[114:117], v5, %[rs], %[so] offen offset:512 sc1\n s_mov_b64 exec, -1\n" \
  "v_exp_f32 v30, v30\n" \
  "s_add_u32 %[ti], %[ti], %[st]\n" \
  "v_exp_f32 v31, v31\n" \
  "s_and_b32 %[ti], %[ti], 0x1ffff\n" \
  "v_exp_f32 v32, v32\n" \
  "s_add_u32 %[so], %[ti], %[hb]\n" \
  "v_exp_f32 v33, v33\n" \
  "v_exp_f32 v34, v34\n" \
  "v_smfmac_f32_32x32x32_bf16 v[56:71], v[118:121], v[24:31], v6\n" \
  "v_exp_f32 v35, v35\n" \
  "v_exp_f32 v36, v36\n" \
  "v_exp_f32 v37, v37\n" \
  "v_exp_f32 v38, v38\n" \
  "v_exp_f32 v39, v39\n" \
  "v_exp_f32 v40, v40\n" \
  "v_exp_f32 v41, v41\n" \
  "v_smfmac_f32_32x32x32_bf16 v[56:71], v[122:125], v[32:39], v6\n" \
  "v_exp_f32 v42, v42\n" \
  "v_exp_f32 v43, v43\n" \
  "v_exp_f32 v44, v44\n" \
  "v_mfma_f32_32x32x16_bf16 v[24:39], v[106:109], v[16:19], 0\n" \
  "v_exp_f32 v45, v45\n" \
  "buffer_store_dwordx4 v[138:141], v4, %[rs], %[td3] offen sc1\n" \
  "v_exp_f32 v46, v46\n" \
  "v_exp_f32 v47, v47\n" \
  "v_exp_f32 v48, v48\n" \
  "v_exp_f32 v49, v49\n" \
  "v_exp_f32 v50, v50\n" \
  "v_smfmac_f32_32x32x32_bf16 v[66:81], v[118:121], v[40:47], v6\n" \
  "v_exp_f32 v51, v51\n" \
  "v_exp_f32 v52, v52\n" \
  "v_exp_f32 v53, v53\n" \
  "v_exp_f32 v54, v54\n" \
  "v_exp_f32 v55, v55\n" \
  "v_exp_f32 v24, v24\n" \
  "v_exp_f32 v25, v25\n" \
  "v_smfmac_f32_32x32x32_bf16 v[66:81], v[122:125], v[48:55], v6\n" \
  "v_exp_f32 v26, v26\n" \
  "v_exp_f32 v27, v27\n" \
  "v_exp_f32 v28, v28\n" \
  "v_mfma_f32_32x32x16_bf16 v[40:55], v[106:109], v[20:23], 0\n" \
  "v_exp_f32 v29, v29\n" \
  "buffer_store_dwordx4 v[142:145], v4, %[rs], %[fo] offen sc1\n" \
  "v_exp_f32 v30, v30\n" \
  "s_add_u32 %[fo], %[fo], 0x200000\n" \
  "v_exp_f32 v31, v31\n" \
  "v_exp_f32 v32, v32\n" \
  "v_exp_f32 v33, v33\n" \
  "v_exp_f32 v34, v34\n" \
  "v_smfmac_f32_32x32x32_bf16 v[76:91], v[118:121], v[24:31], v6\n" \
  "v_exp_f32 v35, v35\n" \
  "v_exp_f32 v36, v36\n" \
  "v_exp_f32 v37, v37\n" \
  "v_exp_f32 v38, v38\n" \
  "v_exp_f32 v39, v39\n" \
  "v_exp_f32 v40, v40\n" \
  "v_exp_f32 v41, v41\n" \
  "v_smfmac_f32_32x32x32_bf16 v[76:91], v[122:125], v[32:39], v6\n" \
  "v_exp_f32 v42, v42\n" \
  "v_exp_f32 v43, v43\n" \
  "v_exp_f32 v44, v44\n" \
  "s_waitcnt vmcnt(2)\n" \
  "v_mfma_f32_32x32x16_bf16 v[24:39], v[102:105], v[8:11], 0\n" \
  "v_exp_f32 v45, v45\n" \
  "v_exp_f32 v46, v46\n" \
  "v_exp_f32 v47, v47\n" \
  "v_exp_f32 v48, v48\n" \
  "v_exp_f32 v49, v49\n" \
  "v_exp_f32 v50, v50\n" \
  "v_smfmac_f32_32x32x32_bf16 v[86:101], v[118:121], v[40:47], v6\n" \
  "v_exp_f32 v51, v51\n" \
  "v_exp_f32 v52, v52\n" \
  "v_exp_f32 v53, v53\n" \
  "v_exp_f32 v54, v54\n" \
  "v_exp_f32 v55, v55\n" \
  "s_barrier\n" \
  "v_exp_f32 v24, v24\n" \
  "v_exp_f32 v25, v25\n" \
  "v_smfmac_f32_32x32x32_bf16 v[86:101], v[122:125], v[48:55], v6\n" \
  "v_exp_f32 v26, v26\n" \
  "v_exp_f32 v27, v27\n" \
  "v_exp_f32 v28, v28\n" \
  "v_mfma_f32_32x32x16_bf16 v[40:55], v[102:105], v[12:15], 0\n" \
  "v_exp_f32 v29, v29\n" \
  "buffer_load_dwordx4 v[106:109], v4, %[rs], %[so] offen sc1\n s_mov_b64 exec, %[vm]\n buffer_load_dwordx4 v[118:121], v5, %[rs], %[so] offen sc1\n buffer_load_dwordx4 v[122:125], v5, %[rs], %[so] offen offset:512 sc1\n s_mov_b64 exec, -1\n" \
  "v_exp_f32 v30, v30\n" \
  "s_add_u32 %[ti], %[ti], %[st]\n" \
  "v_exp_f32 v31, v31\n" \
  "s_and_b32 %[ti], %[ti], 0x1ffff\n" \
  "v_exp_f32 v32, v32\n" \
  "s_add_u32 %[so], %[ti], %[hb]\n" \
  "v_exp_f32 v33, v33\n" \
  "v_exp_f32 v34, v34\n" \
  "v_smfmac_f32_32x32x32_bf16 v[56:71], v[110:113], v[24:31], v6\n" \
  "v_exp_f32 v35, v35\n" \
  "v_exp_f32 v36, v36\n" \
  "v_exp_f32 v37, v37\n" \
  "v_exp_f32 v38, v38\n" \
  "v_exp_f32 v39, v39\n" \
  "v_exp_f32 v40, v40\n" \
  "v_exp_f32 v41, v41\n" \
  "v_smfmac_f32_32x32x32_bf16 v[56:71], v[114:117], v[32:39], v6\n" \
  "v_exp_f32 v42, v42\n" \
  "v_exp_f32 v43, v43\n" \
  "v_exp_f32 v44, v44\n" \
  "v_mfma_f32_32x32x16_bf16 v[24:39], v[102:105], v[16:19], 0\n" \
  "v_exp_f32 v45, v45\n" \
  "buffer_store_dwordx4 v[142:145], %[vx], %[rs], %[fx] offen sc1\n" \
  "v_exp_f32 v46, v46\n" \
  "s_add_u32 %[fx], %[fx], 0x200000\n" \
  "v_exp_f32 v47, v47\n" \
  "v_exp_f32 v48, v48\n" \
  "v_exp_f32 v49, v49\n" \
  "v_exp_f32 v50, v50\n" \
  "v_smfmac_f32_32x32x32_bf16 v[66:81], v[110:113], v[40:47], v6\n" \
  "v_exp_f32 v51, v51\n" \
  "v_exp_f32 v52, v52\n" \
  "v_exp_f32 v53, v53\n" \
  "v_exp_f32 v54, v54\n" \
  "v_exp_f32 v55, v55\n" \
  "v_exp_f32 v24, v24\n" \
  "v_exp_f32 v25, v25\n" \
  "v_smfmac_f32_32x32x32_bf16 v[66:81], v[114:117], v[48:55], v6\n" \
  "v_exp_f32 v26, v26\n" \
  "v_exp_f32 v27, v27\n" \
  "v_exp_f32 v28, v28\n" \
  "v_mfma_f32_32x32x16_bf16 v[40:55], v[102:105], v[20:23], 0\n" \
  "v_exp_f32 v29, v29\n" \
  "v_exp_f32 v30, v30\n" \
  "v_exp_f32 v31, v31\n" \
  "v_exp_f32 v32, v32\n" \
  "v_exp_f32 v33, v33\n" \
  "v_exp_f32 v34, v34\n" \
  "v_smfmac_f32_32x32x32_bf16 v[76:91], v[110:113], v[24:31], v6\n" \
  "v_exp_f32 v35, v35\n" \
  "v_exp_f32 v36, v36\n" \
  "v_exp_f32 v37, v37\n" \
  "v_exp_f32 v38, v38\n" \
  "v_exp_f32 v39, v39\n" \
  "v_exp_f32 v40, v40\n" \
  "v_exp_f32 v41, v41\n" \
  "v_smfmac_f32_32x32x32_bf16 v[76:91], v[114:117], v[32:39], v6\n" \
  "v_exp_f32 v42, v42\n" \
  "v_exp_f32 v43, v43\n" \
  "v_exp_f32 v44, v44\n" \
  "s_waitcnt vmcnt(1)\n" \
  "v_mfma_f32_32x32x16_bf16 v[24:39], v[106:109], v[8:11], 0\n" \
  "v_exp_f32 v45, v45\n" \
  "v_exp_f32 v46, v46\n" \
  "v_exp_f32 v47, v47\n" \
  "v_exp_f32 v48, v48\n" \
  "v_exp_f32 v49, v49\n" \
  "v_exp_f32 v50, v50\n" \
  "v_smfmac_f32_32x32x32_bf16 v[86:101], v[110:113], v[40:47], v6\n" \
  "v_exp_f32 v51, v51\n" \
  "v_exp_f32 v52, v52\n" \
  "v_exp_f32 v53, v53\n" \
  "v_exp_f32 v54, v54\n" \
  "v_exp_f32 v55, v55\n" \
  "v_exp_f32 v24, v24\n" \
  "v_exp_f32 v25, v25\n" \
  "v_smfmac_f32_32x32x32_bf16 v[86:101], v[114:117], v[48:55], v6\n" \
  "v_exp_f32 v26, v26\n" \
  "v_exp_f32 v27, v27\n" \
  "v_exp_f32 v28, v28\n" \
  "v_mfma_f32_32x32x16_bf16 v[40:55], v[106:109], v[12:15], 0\n" \
  "v_exp_f32 v29, v29\n" \
  "buffer_load_dwordx4 v[102:105], v4, %[rs], %[so] offen sc1\n s_mov_b64 exec, %[vm]\n buffer_load_dwordx4 v[110:113], v5, %[rs], %[so] offen sc1\n buffer_load_dwordx4 v[114:117], v5, %[rs], %[so] offen offset:512 sc1\n s_mov_b64 exec, -1\n" \
  "v_exp_f32 v30, v30\n" \
  "s_add_u32 %[ti], %[ti], %[st]\n" \
  "v_exp_f32 v31, v31\n" \
  "s_and_b32 %[ti], %[ti], 0x1ffff\n" \
  "v_exp_f32 v32, v32\n" \
  "s_add_u32 %[so], %[ti], %[hb]\n" \
  "v_exp_f32 v33, v33\n" \
  "v_exp_f32 v34, v34\n" \
  "v_smfmac_f32_32x32x32_bf16 v[56:71], v[118:121], v[24:31], v6\n" \
  "v_exp_f32 v35, v35\n" \
  "v_exp_f32 v36, v36\n" \
  "v_exp_f32 v37, v37\n" \
  "v_exp_f32 v38, v38\n" \
  "v_exp_f32 v39, v39\n" \
  "v_exp_f32 v40, v40\n" \
  "v_exp_f32 v41, v41\n" \
  "v_smfmac_f32_32x32x32_bf16 v[56:71], v[122:125], v[32:39], v6\n" \
  "v_exp_f32 v42, v42\n" \
  "v_exp_f32 v43, v43\n" \
  "v_exp_f32 v44, v44\n" \
  "v_mfma_f32_32x32x16_bf16 v[24:39], v[106:109], v[16:19], 0\n" \
  "v_exp_f32 v45, v45\n" \
  "v_exp_f32 v46, v46\n" \
  "v_exp_f32 v47, v47\n" \
  "v_exp_f32 v48, v48\n" \
  "v_exp_f32 v49, v49\n" \
  "v_exp_f32 v50, v50\n" \
  "v_smfmac_f32_32x32x32_bf16 v[66:81], v[118:121], v[40:47], v6\n" \
  "v_exp_f32 v51, v51\n" \
  "v_exp_f32 v52, v52\n" \
  "v_exp_f32 v53, v53\n" \
  "v_exp_f32 v54, v54\n" \
  "v_exp_f32 v55, v55\n" \
  "v_exp_f32 v24, v24\n" \
  "v_exp_f32 v25, v25\n" \
  "v_smfmac_f32_32x32x32_bf16 v[66:81], v[122:125], v[48:55], v6\n" \
  "v_exp_f32 v26, v26\n" \
  "v_exp_f32 v27, v27\n" \
  "v_exp_f32 v28, v28\n" \
  "v_mfma_f32_32x32x16_bf16 v[40:55], v[106:109], v[20:23], 0\n" \
  "v_exp_f32 v29, v29\n" \
  "v_exp_f32 v30, v30\n" \
  "v_exp_f32 v31, v31\n" \
  "v_exp_f32 v32, v32\n" \
  "v_exp_f32 v33, v33\n" \
  "v_exp_f32 v34, v34\n" \
  "v_smfmac_f32_32x32x32_bf16 v[76:91], v[118:121], v[24:31], v6\n" \
  "v_exp_f32 v35, v35\n" \
  "v_exp_f32 v36, v36\n" \
  "v_exp_f32 v37, v37\n" \
  "v_exp_f32 v38, v38\n" \
  "v_exp_f32 v39, v39\n" \
  "v_exp_f32 v40, v40\n" \
  "v_exp_f32 v41, v41\n" \
  "v_smfmac_f32_32x32x32_bf16 v[76:91], v[122:125], v[32:39], v6\n" \
  "v_exp_f32 v42, v42\n" \
  "v_exp_f32 v43, v43\n" \
  "v_exp_f32 v44, v44\n" \
  "s_waitcnt vmcnt(0)\n" \
  "v_mfma_f32_32x32x16_bf16 v[24:39], v[102:105], v[8:11], 0\n" \
  "v_exp_f32 v45, v45\n" \
  "v_exp_f32 v46, v46\n" \
  "v_exp_f32 v47, v47\n" \
  "v_exp_f32 v48, v48\n" \
  "v_exp_f32 v49, v49\n" \
  "v_exp_f32 v50, v50\n" \
  "v_smfmac_f32_32x32x32_bf16 v[86:101], v[118:121], v[40:47], v6\n" \
  "v_exp_f32 v51, v51\n" \
  "v_exp_f32 v52, v52\n" \
  "v_exp_f32 v53, v53\n" \
  "v_exp_f32 v54, v54\n" \
  "v_exp_f32 v55, v55\n" \
  "s_barrier\n" \
  "s_sub_u32 %[n], %[n], 3\n" \
  "s_cmp_lg_u32 %[wv], 0\n" \
  "s_cbranch_scc1 1f\n" \
  "s_mov_b64 exec, 1\n" \
  "buffer_store_dwordx2 v[146:147], off, %[rs], %[sp] sc1\n" \
  "s_mov_b64 exec, -1\n" \
  "1:\n" \
  "v_exp_f32 v24, v24\n" \
  "v_exp_f32 v25, v25\n" \
  "v_smfmac_f32_32x32x32_bf16 v[86:101], v[122:125], v[48:55], v6\n" \
  "v_exp_f32 v26, v26\n" \
  "v_exp_f32 v27, v27\n" \
  "v_exp_f32 v28, v28\n" \
  "v_mfma_f32_32x32x16_bf16 v[40:55], v[102:105], v[12:15], 0\n" \
  "v_exp_f32 v29, v29\n" \
  "buffer_load_dwordx4 v[106:109], v4, %[rs], %[so] offen sc1\n s_mov_b64 exec, %[vm]\n buffer_load_dwordx4 v[118:121], v5, %[rs], %[so] offen sc1\n buffer_load_dwordx4 v[122:125], v5, %[rs], %[so] offen offset:512 sc1\n s_mov_b64 exec, -1\n" \
  "v_exp_f32 v30, v30\n" \
  "s_add_u32 %[ti], %[ti], %[st]\n" \
  "v_exp_f32 v31, v31\n" \
  "s_and_b32 %[ti], %[ti], 0x1ffff\n" \
  "v_exp_f32 v32, v32\n" \
  "s_add_u32 %[so], %[ti], %[hb]\n" \
  "v_exp_f32 v33, v33\n" \
  "v_exp_f32 v34, v34\n" \
  "v_smfmac_f32_32x32x32_bf16 v[56:71], v[110:113], v[24:31], v6\n" \
  "v_exp_f32 v35, v35\n" \
  "v_exp_f32 v36, v36\n" \
  "v_exp_f32 v37, v37\n" \
  "v_exp_f32 v38, v38\n" \
  "v_exp_f32 v39, v39\n" \
  "v_exp_f32 v40, v40\n" \
  "v_exp_f32 v41, v41\n" \
  "v_smfmac_f32_32x32x32_bf16 v[56:71], v[114:117], v[32:39], v6\n" \
  "v_exp_f32 v42, v42\n" \
  "v_exp_f32 v43, v43\n" \
  "v_exp_f32 v44, v44\n" \
  "v_mfma_f32_32x32x16_bf16 v[24:39], v[102:105], v[16:19], 0\n" \
  "v_exp_f32 v45, v45\n" \
  "v_exp_f32 v46, v46\n" \
  "v_exp_f32 v47, v47\n" \
  "v_exp_f32 v48, v48\n" \
  "v_exp_f32 v49, v49\n" \
  "v_exp_f32 v50, v50\n" \
  "v_smfmac_f32_32x32x32_bf16 v[66:81], v[110:113], v[40:47], v6\n" \
  "v_exp_f32 v51, v51\n" \
  "v_exp_f32 v52, v52\n" \
  "v_exp_f32 v53, v53\n" \
  "v_exp_f32 v54, v54\n" \
  "v_exp_f32 v55, v55\n" \
  "v_exp_f32 v24, v24\n" \
  "v_exp_f32 v25, v25\n" \
  "v_smfmac_f32_32x32x32_bf16 v[66:81], v[114:117], v[48:55], v6\n" \
  "v_exp_f32 v26, v26\n" \
  "v_exp_f32 v27, v27\n" \
  "v_exp_f32 v28, v28\n" \
  "v_mfma_f32_32x32x16_bf16 v[40:55], v[102:105], v[20:23], 0\n" \
  "v_exp_f32 v29, v29\n" \
  "v_exp_f32 v30, v30\n" \
  "v_exp_f32 v31, v31\n" \
  "v_exp_f32 v32, v32\n" \
  "v_exp_f32 v33, v33\n" \
  "v_exp_f32 v34, v34\n" \
  "v_smfmac_f32_32x32x32_bf16 v[76:91], v[110:113], v[24:31], v6\n" \
  "v_exp_f32 v35, v35\n" \
  "v_exp_f32 v36, v36\n" \
  "v_exp_f32 v37, v37\n" \
  "v_exp_f32 v38, v38\n" \
  "v_exp_f32 v39, v39\n" \
  "v_exp_f32 v40, v40\n" \
  "v_exp_f32 v41, v41\n" \
  "v_smfmac_f32_32x32x32_bf16 v[76:91], v[114:117], v[32:39], v6\n" \
  "v_exp_f32 v42, v42\n" \
  "v_exp_f32 v43, v43\n" \
  "v_exp_f32 v44, v44\n" \
  "s_waitcnt vmcnt(0)\n" \
  "v_mfma_f32_32x32x16_bf16 v[24:39], v[106:109], v[8:11], 0\n" \
  "v_exp_f32 v45, v45\n" \
  "v_exp_f32 v46, v46\n" \
  "v_exp_f32 v47, v47\n" \
  "v_exp_f32 v48, v48\n" \
  "v_exp_f32 v49, v49\n" \
  "v_exp_f32 v50, v50\n" \
  "v_smfmac_f32_32x32x32_bf16 v[86:101], v[110:113], v[40:47], v6\n" \
  "v_exp_f32 v51, v51\n" \
  "v_exp_f32 v52, v52\n" \
  "v_exp_f32 v53, v53\n" \
  "v_exp_f32 v54, v54\n" \
  "v_exp_f32 v55, v55\n" \
  "v_exp_f32 v24, v24\n" \
  "v_exp_f32 v25, v25\n" \
  "v_smfmac_f32_32x32x32_bf16 v[86:101], v[114:117], v[48:55], v6\n" \
  "v_exp_f32 v26, v26\n" \
  "v_exp_f32 v27, v27\n" \
  "v_exp_f32 v28, v28\n" \
  "v_mfma_f32_32x32x16_bf16 v[40:55], v[106:109], v[12:15], 0\n" \
  "v_exp_f32 v29, v29\n" \
  "buffer_load_dwordx4 v[102:105], v4, %[rs], %[so] offen sc1\n s_mov_b64 exec, %[vm]\n buffer_load_dwordx4 v[110:113], v5, %[rs], %[so] offen sc1\n buffer_load_dwordx4 v[114:117], v5, %[rs], %[so] offen offset:512 sc1\n s_mov_b64 exec, -1\n" \
  "v_exp_f32 v30, v30\n" \
  "s_add_u32 %[ti], %[ti], %[st]\n" \
  "v_exp_f32 v31, v31\n" \
  "s_and_b32 %[ti], %[ti], 0x1ffff\n" \
  "v_exp_f32 v32, v32\n" \
  "s_add_u32 %[so], %[ti], %[hb]\n" \
  "v_exp_f32 v33, v33\n" \
  "v_exp_f32 v34, v34\n" \
  "v_smfmac_f32_32x32x32_bf16 v[56:71], v[118:121], v[24:31], v6\n" \
  "v_exp_f32 v35, v35\n" \
  "v_exp_f32 v36, v36\n" \
  "v_exp_f32 v37, v37\n" \
  "v_exp_f32 v38, v38\n" \
  "v_exp_f32 v39, v39\n" \
  "v_exp_f32 v40, v40\n" \
  "v_exp_f32 v41, v41\n" \
  "v_smfmac_f32_32x32x32_bf16 v[56:71], v[122:125], v[32:39], v6\n" \
  "v_exp_f32 v42, v42\n" \
  "v_exp_f32 v43, v43\n" \
  "v_exp_f32 v44, v44\n" \
  "v_mfma_f32_32x32x16_bf16 v[24:39], v[106:109], v[16:19], 0\n" \
  "v_exp_f32 v45, v45\n" \
  "v_exp_f32 v46, v46\n" \
  "v_exp_f32 v47, v47\n" \
  "v_exp_f32 v48, v48\n" \
  "v_exp_f32 v49, v49\n" \
  "v_exp_f32 v50, v50\n" \
  "v_smfmac_f32_32x32x32_bf16 v[66:81], v[118:121], v[40:47], v6\n" \
  "v_exp_f32 v51, v51\n" \
  "v_exp_f32 v52, v52\n" \
  "v_exp_f32 v53, v53\n" \
  "v_exp_f32 v54, v54\n" \
  "v_exp_f32 v55, v55\n" \
  "v_exp_f32 v24, v24\n" \
  "v_exp_f32 v25, v25\n" \
  "v_smfmac_f32_32x32x32_bf16 v[66:81], v[122:125], v[48:55], v6\n" \
  "v_exp_f32 v26, v26\n" \
  "v_exp_f32 v27, v27\n" \
  "v_exp_f32 v28, v28\n" \
  "v_mfma_f32_32x32x16_bf16 v[40:55], v[106:109], v[20:23], 0\n" \
  "v_exp_f32 v29, v29\n" \
  "v_exp_f32 v30, v30\n" \
  "v_exp_f32 v31, v31\n" \
  "v_exp_f32 v32, v32\n" \
  "v_exp_f32 v33, v33\n" \
  "v_exp_f32 v34, v34\n" \
  "v_smfmac_f32_32x32x32_bf16 v[76:91], v[118:121], v[24:31], v6\n" \
  "v_exp_f32 v35, v35\n" \
  "v_exp_f32 v36, v36\n" \
  "v_exp_f32 v37, v37\n" \
  "v_exp_f32 v38, v38\n" \
  "v_exp_f32 v39, v39\n" \
  "v_exp_f32 v40, v40\n" \
  "v_exp_f32 v41, v41\n" \
  "v_smfmac_f32_32x32x32_bf16 v[76:91], v[122:125], v[32:39], v6\n" \
  "v_exp_f32 v42, v42\n" \
  "v_exp_f32 v43, v43\n" \
  "v_exp_f32 v44, v44\n" \
  "s_waitcnt vmcnt(0)\n" \
  "v_mfma_f32_32x32x16_bf16 v[24:39], v[102:105], v[8:11], 0\n" \
  "v_exp_f32 v45, v45\n" \
  "v_exp_f32 v46, v46\n" \
  "v_exp_f32 v47, v47\n" \
  "v_exp_f32 v48, v48\n" \
  "v_exp_f32 v49, v49\n" \
  "v_exp_f32 v50, v50\n" \
  "v_smfmac_f32_32x32x32_bf16 v[86:101], v[118:121], v[40:47], v6\n" \
  "v_exp_f32 v51, v51\n" \
  "v_exp_f32 v52, v52\n" \
  "v_exp_f32 v53, v53\n" \
  "v_exp_f32 v54, v54\n" \
  "v_exp_f32 v55, v55\n" \
  "s_sub_u32 %[n], %[n], 1\n" \
  "s_cmp_lg_u32 %[n], 0\n" \
  "s_cbranch_scc1 1b\n" \
  "s_cmp_eq_u32 %[gc], 0\n" \
  "s_cbranch_scc1 3f\n" \
  "2:\n" \
  "buffer_load_dwordx4 v[126:129], v4, %[rs], %[gs] offen sc1\n" \
  "buffer_load_dwordx4 v[130:133], v4, %[rs], %[gs] offen offset:1024 sc1\n" \
  "s_sub_u32 %[gc], %[gc], 1\n" \
  "s_waitcnt vmcnt(0)\n" \
  "v_cmp_ne_u64 vcc, %[tg], v[126:127]\n" \
  "s_mov_b64 %[tmp], vcc\n" \
  "v_cmp_ne_u64 vcc, %[tg], v[128:129]\n" \
  "s_or_b64 %[tmp], %[tmp], vcc\n" \
  "v_cmp_ne_u64 vcc, %[tg], v[130:131]\n" \
  "s_or_b64 %[tmp], %[tmp], vcc\n" \
  "v_cmp_ne_u64 vcc, %[tg], v[132:133]\n" \
  "s_or_b64 %[tmp], %[tmp], vcc\n" \
  "s_cbranch_scc0 3f\n" \
  "s_cmp_lg_u32 %[gc], 0\n" \
  "s_cbranch_scc1 2b\n" \
  "3:\n" \
  "v_exp_f32 v24, v24\n" \
  "v_exp_f32 v25, v25\n" \
  "v_smfmac_f32_32x32x32_bf16 v[86:101], v[122:125], v[48:55], v6\n" \
  "v_exp_f32 v26, v26\n" \
  "v_exp_f32 v27, v27\n" \
  "v_exp_f32 v28, v28\n" \
  "v_mfma_f32_32x32x16_bf16 v[40:55], v[102:105], v[12:15], 0\n" \
  "v_exp_f32 v29, v29\n" \
  "buffer_load_dwordx4 v[106:109], v4, %[rs], %[so] offen sc1\n s_mov_b64 exec, %[vm]\n buffer_load_dwordx4 v[118:121], v5, %[rs], %[so] offen sc1\n buffer_load_dwordx4 v[122:125], v5, %[rs], %[so] offen offset:512 sc1\n s_mov_b64 exec, -1\n" \
  "v_exp_f32 v30, v30\n" \
  "s_add_u32 %[ti], %[ti], %[st]\n" \
  "v_exp_f32 v31, v31\n" \
  "s_and_b32 %[ti], %[ti], 0x1ffff\n" \
  "v_exp_f32 v32, v32\n" \
  "s_add_u32 %[so], %[ti], %[hb]\n" \
  "v_exp_f32 v33, v33\n" \
  "v_exp_f32 v34, v34\n" \
  "v_smfmac_f32_32x32x32_bf16 v[56:71], v[110:113], v[24:31], v6\n" \
  "v_exp_f32 v35, v35\n" \
  "v_exp_f32 v36, v36\n" \
  "v_exp_f32 v37, v37\n" \
  "v_exp_f32 v38, v38\n" \
  "v_exp_f32 v39, v39\n" \
  "v_exp_f32 v40, v40\n" \
  "v_exp_f32 v41, v41\n" \
  "v_smfmac_f32_32x32x32_bf16 v[56:71], v[114:117], v[32:39], v6\n" \
  "v_exp_f32 v42, v42\n" \
  "v_exp_f32 v43, v43\n" \
  "v_exp_f32 v44, v44\n" \
  "v_mfma_f32_32x32x16_bf16 v[24:39], v[102:105], v[16:19], 0\n" \
  "v_exp_f32 v45, v45\n" \
  "buffer_load_dwordx4 v[126:129], v4, %[rs], %[pw] offen offset:0 sc1\n buffer_load_dwordx4 v[130:133], v4, %[rs], %[pw] offen offset:1024 sc1\n buffer_load_dwordx4 v[134:137], v4, %[rs], %[pw] offen offset:2048 sc1\n" \
  "v_exp_f32 v46, v46\n" \
  "v_exp_f32 v47, v47\n" \
  "v_exp_f32 v48, v48\n" \
  "v_exp_f32 v49, v49\n" \
  "v_exp_f32 v50, v50\n" \
  "v_smfmac_f32_32x32x32_bf16 v[66:81], v[110:113], v[40:47], v6\n" \
  "v_exp_f32 v51, v51\n" \
  "v_exp_f32 v52, v52\n" \
  "v_exp_f32 v53, v53\n" \
  "v_exp_f32 v54, v54\n" \
  "v_exp_f32 v55, v55\n" \
  "v_exp_f32 v24, v24\n" \
  "v_exp_f32 v25, v25\n" \
  "v_smfmac_f32_32x32x32_bf16 v[66:81], v[114:117], v[48:55], v6\n" \
  "v_exp_f32 v26, v26\n" \
  "v_exp_f32 v27, v27\n" \
  "v_exp_f32 v28, v28\n" \
  "v_mfma_f32_32x32x16_bf16 v[40:55], v[102:105], v[20:23], 0\n" \
  "v_exp_f32 v29, v29\n" \
  "buffer_load_dwordx4 v[138:141], v4, %[rs], %[pw] offen offset:3072 sc1\n s_add_u32 %[pw], %[pw], 0x1000\n buffer_load_dwordx4 v[142:145], v4, %[rs], %[pw] offen offset:0 sc1\n buffer_load_dwordx4 v[146:149], v4, %[rs], %[pw] offen offset:1024 sc1\n" \
  "v_exp_f32 v30, v30\n" \
  "v_exp_f32 v31, v31\n" \
  "v_exp_f32 v32, v32\n" \
  "v_exp_f32 v33, v33\n" \
  "v_exp_f32 v34, v34\n" \
  "v_smfmac_f32_32x32x32_bf16 v[76:91], v[110:113], v[24:31], v6\n" \
  "v_exp_f32 v35, v35\n" \
  "v_exp_f32 v36, v36\n" \
  "v_exp_f32 v37, v37\n" \
  "v_exp_f32 v38, v38\n" \
  "v_exp_f32 v39, v39\n" \
  "v_exp_f32 v40, v40\n" \
  "v_exp_f32 v41, v41\n" \
  "v_smfmac_f32_32x32x32_bf16 v[76:91], v[114:117], v[32:39], v6\n" \
  "v_exp_f32 v42, v42\n" \
  "v_exp_f32 v43, v43\n" \
  "v_exp_f32 v44, v44\n" \
  "s_waitcnt vmcnt(6)\n" \
  "v_mfma_f32_32x32x16_bf16 v[24:39], v[106:109], v[8:11], 0\n" \
  "v_exp_f32 v45, v45\n" \
  "v_exp_f32 v46, v46\n" \
  "v_exp_f32 v47, v47\n" \
  "v_exp_f32 v48, v48\n" \
  "v_exp_f32 v49, v49\n" \
  "v_exp_f32 v50, v50\n" \
  "v_smfmac_f32_32x32x32_bf16 v[86:101], v[110:113], v[40:47], v6\n" \
  "v_exp_f32 v51, v51\n" \
  "v_exp_f32 v52, v52\n" \
  "v_exp_f32 v53, v53\n" \
  "v_exp_f32 v54, v54\n" \
  "v_exp_f32 v55, v55\n" \
  "v_exp_f32 v24, v24\n" \
  "v_exp_f32 v25, v25\n" \
  "v_smfmac_f32_32x32x32_bf16 v[86:101], v[114:117], v[48:55], v6\n" \
  "v_exp_f32 v26, v26\n" \
  "v_exp_f32 v27, v27\n" \
  "v_exp_f32 v28, v28\n" \
  "v_mfma_f32_32x32x16_bf16 v[40:55], v[106:109], v[12:15], 0\n" \
  "v_exp_f32 v29, v29\n" \
  "buffer_load_dwordx4 v[102:105], v4, %[rs], %[so] offen sc1\n s_mov_b64 exec, %[vm]\n buffer_load_dwordx4 v[110:113], v5, %[rs], %[so] offen sc1\n buffer_load_dwordx4 v[114:117], v5, %[rs], %[so] offen offset:512 sc1\n s_mov_b64 exec, -1\n" \
  "v_exp_f32 v30, v30\n" \
  "s_add_u32 %[ti], %[ti], %[st]\n" \
  "v_exp_f32 v31, v31\n" \
  "s_and_b32 %[ti], %[ti], 0x1ffff\n" \
  "v_exp_f32 v32, v32\n" \
  "s_add_u32 %[so], %[ti], %[hb]\n" \
  "v_exp_f32 v33, v33\n" \
  "v_exp_f32 v34, v34\n" \
  "v_smfmac_f32_32x32x32_bf16 v[56:71], v[118:121], v[24:31], v6\n" \
  "v_exp_f32 v35, v35\n" \
  "v_exp_f32 v36, v36\n" \
  "v_exp_f32 v37, v37\n" \
  "v_exp_f32 v38, v38\n" \
  "v_exp_f32 v39, v39\n" \
  "v_exp_f32 v40, v40\n" \
  "v_exp_f32 v41, v41\n" \
  "v_smfmac_f32_32x32x32_bf16 v[56:71], v[122:125], v[32:39], v6\n" \
  "v_exp_f32 v42, v42\n" \
  "v_exp_f32 v43, v43\n" \
  "v_exp_f32 v44, v44\n" \
  "v_mfma_f32_32x32x16_bf16 v[24:39], v[106:109], v[16:19], 0\n" \
  "v_exp_f32 v45, v45\n" \
  "buffer_load_dwordx4 %[w6], v4, %[rs], %[pw] offen offset:2048 sc1\n buffer_load_dwordx4 %[w7], v4, %[rs], %[pw] offen offset:3072 sc1\n s_add_u32 %[pw], %[pw], 0x1000\n buffer_load_dwordx4 %[w8], v4, %[rs], %[pw] offen offset:0 sc1\n" \
  "v_exp_f32 v46, v46\n" \
  "v_exp_f32 v47, v47\n" \
  "v_exp_f32 v48, v48\n" \
  "v_exp_f32 v49, v49\n" \
  "v_exp_f32 v50, v50\n" \
  "v_smfmac_f32_32x32x32_bf16 v[66:81], v[118:121], v[40:47], v6\n" \
  "v_exp_f32 v51, v51\n" \
  "v_exp_f32 v52, v52\n" \
  "v_exp_f32 v53, v53\n" \
  "v_exp_f32 v54, v54\n" \
  "v_exp_f32 v55, v55\n" \
  "v_exp_f32 v24, v24\n" \
  "v_exp_f32 v25, v25\n" \
  "v_smfmac_f32_32x32x32_bf16 v[66:81], v[122:125], v[48:55], v6\n" \
  "v_exp_f32 v26, v26\n" \
  "v_exp_f32 v27, v27\n" \
  "v_exp_f32 v28, v28\n" \
  "v_mfma_f32_32x32x16_bf16 v[40:55], v[106:109], v[20:23], 0\n" \
  "v_exp_f32 v29, v29\n" \
  "buffer_load_dwordx4 %[w9], v4, %[rs], %[pw] offen offset:1024 sc1\n buffer_load_dwordx4 %[w10], v4, %[rs], %[pw] offen offset:2048 sc1\n buffer_load_dwordx4 %[w11], v4, %[rs], %[pw] offen offset:3072 sc1\n s_add_u32 %[pw], %[pw], 0x1000\n" \
  "v_exp_f32 v30, v30\n" \
  "v_exp_f32 v31, v31\n" \
  "v_exp_f32 v32, v32\n" \
  "v_exp_f32 v33, v33\n" \
  "v_exp_f32 v34, v34\n" \
  "v_smfmac_f32_32x32x32_bf16 v[76:91], v[118:121], v[24:31], v6\n" \
  "v_exp_f32 v35, v35\n" \
  "v_exp_f32 v36, v36\n" \
  "v_exp_f32 v37, v37\n" \
  "v_exp_f32 v38, v38\n" \
  "v_exp_f32 v39, v39\n" \
  "v_exp_f32 v40, v40\n" \
  "v_exp_f32 v41, v41\n" \
  "v_smfmac_f32_32x32x32_bf16 v[76:91], v[122:125], v[32:39], v6\n" \
  "v_exp_f32 v42, v42\n" \
  "v_exp_f32 v43, v43\n" \
  "v_exp_f32 v44, v44\n" \
  "s_waitcnt vmcnt(6)\n" \
  "v_mfma_f32_32x32x16_bf16 v[24:39], v[102:105], v[8:11], 0\n" \
  "v_exp_f32 v45, v45\n" \
  "v_exp_f32 v46, v46\n" \
  "v_exp_f32 v47, v47\n" \
  "v_exp_f32 v48, v48\n" \
  "v_exp_f32 v49, v49\n" \
  "v_exp_f32 v50, v50\n" \
  "v_smfmac_f32_32x32x32_bf16 v[86:101], v[118:121], v[40:47], v6\n" \
  "v_exp_f32 v51, v51\n" \
  "v_exp_f32 v52, v52\n" \
  "v_exp_f32 v53, v53\n" \
  "v_exp_f32 v54, v54\n" \
  "v_exp_f32 v55, v55\n" \
  "v_exp_f32 v24, v24\n" \
  "v_exp_f32 v25, v25\n" \
  "v_smfmac_f32_32x32x32_bf16 v[86:101], v[122:125], v[48:55], v6\n" \
  "v_exp_f32 v26, v26\n" \
  "v_exp_f32 v27, v27\n" \
  "v_exp_f32 v28, v28\n" \
  "v_mfma_f32_32x32x16_bf16 v[40:55], v[102:105], v[12:15], 0\n" \
  "v_exp_f32 v29, v29\n" \
  "buffer_load_dwordx4 v[106:109], v4, %[rs], %[so] offen sc1\n s_mov_b64 exec, %[vm]\n buffer_load_dwordx4 v[118:121], v5, %[rs], %[so] offen sc1\n buffer_load_dwordx4 v[122:125], v5, %[rs], %[so] offen offset:512 sc1\n s_mov_b64 exec, -1\n" \
  "v_exp_f32 v30, v30\n" \
  "s_add_u32 %[ti], %[ti], %[st]\n" \
  "v_exp_f32 v31, v31\n" \
  "s_and_b32 %[ti], %[ti], 0x1ffff\n" \
  "v_exp_f32 v32, v32\n" \
  "s_add_u32 %[so], %[ti], %[hb]\n" \
  "v_exp_f32 v33, v33\n" \
  "v_exp_f32 v34, v34\n" \
  "v_smfmac_f32_32x32x32_bf16 v[56:71], v[110:113], v[24:31], v6\n" \
  "v_exp_f32 v35, v35\n" \
  "v_exp_f32 v36, v36\n" \
  "v_exp_f32 v37, v37\n" \
  "v_exp_f32 v38, v38\n" \
  "v_exp_f32 v39, v39\n" \
  "v_exp_f32 v40, v40\n" \
  "v_exp_f32 v41, v41\n" \
  "v_smfmac_f32_32x32x32_bf16 v[56:71], v[114:117], v[32:39], v6\n" \
  "v_exp_f32 v42, v42\n" \
  "v_exp_f32 v43, v43\n" \
  "v_exp_f32 v44, v44\n" \
  "v_mfma_f32_32x32x16_bf16 v[24:39], v[102:105], v[16:19], 0\n" \
  "v_exp_f32 v45, v45\n" \
  "buffer_load_dwordx4 %[w12], v4, %[rs], %[pw] offen offset:0 sc1\n buffer_load_dwordx4 %[w13], v4, %[rs], %[pw] offen offset:1024 sc1\n buffer_load_dwordx4 %[w14], v4, %[rs], %[pw] offen offset:2048 sc1\n" \
  "v_exp_f32 v46, v46\n" \
  "v_exp_f32 v47, v47\n" \
  "v_exp_f32 v48, v48\n" \
  "v_exp_f32 v49, v49\n" \
  "v_exp_f32 v50, v50\n" \
  "v_smfmac_f32_32x32x32_bf16 v[66:81], v[110:113], v[40:47], v6\n" \
  "v_exp_f32 v51, v51\n" \
  "v_exp_f32 v52, v52\n" \
  "v_exp_f32 v53, v53\n" \
  "v_exp_f32 v54, v54\n" \
  "v_exp_f32 v55, v55\n" \
  "v_exp_f32 v24, v24\n" \
  "v_exp_f32 v25, v25\n" \
  "v_smfmac_f32_32x32x32_bf16 v[66:81], v[114:117], v[48:55], v6\n" \
  "v_exp_f32 v26, v26\n" \
  "v_exp_f32 v27, v27\n" \
  "v_exp_f32 v28, v28\n" \
  "v_mfma_f32_32x32x16_bf16 v[40:55], v[102:105], v[20:23], 0\n" \
  "v_exp_f32 v29, v29\n" \
  "buffer_load_dwordx4 %[w15], v4, %[rs], %[pw] offen offset:3072 sc1\n s_add_u32 %[pw], %[pw], 0x1000\n buffer_load_dwordx4 %[w16], v4, %[rs], %[pw] offen offset:0 sc1\n buffer_load_dwordx4 %[w17], v4, %[rs], %[pw] offen offset:1024 sc1\n" \
  "v_exp_f32 v30, v30\n" \
  "v_exp_f32 v31, v31\n" \
  "v_exp_f32 v32, v32\n" \
  "v_exp_f32 v33, v33\n" \
  "v_exp_f32 v34, v34\n" \
  "v_smfmac_f32_32x32x32_bf16 v[76:91], v[110:113], v[24:31], v6\n" \
  "v_exp_f32 v35, v35\n" \
  "v_exp_f32 v36, v36\n" \
  "v_exp_f32 v37, v37\n" \
  "v_exp_f32 v38, v38\n" \
  "v_exp_f32 v39, v39\n" \
  "v_exp_f32 v40, v40\n" \
  "v_exp_f32 v41, v41\n" \
  "v_smfmac_f32_32x32x32_bf16 v[76:91], v[114:117], v[32:39], v6\n" \
  "v_exp_f32 v42, v42\n" \
  "v_exp_f32 v43, v43\n" \
  "v_exp_f32 v44, v44\n" \
  "s_waitcnt vmcnt(6)\n" \
  "v_mfma_f32_32x32x16_bf16 v[24:39], v[106:109], v[8:11], 0\n" \
  "v_exp_f32 v45, v45\n" \
  "v_exp_f32 v46, v46\n" \
  "v_exp_f32 v47, v47\n" \
  "v_exp_f32 v48, v48\n" \
  "v_exp_f32 v49, v49\n" \
  "v_exp_f32 v50, v50\n" \
  "v_smfmac_f32_32x32x32_bf16 v[86:101], v[110:113], v[40:47], v6\n" \
  "v_exp_f32 v51, v51\n" \
  "v_exp_f32 v52, v52\n" \
  "v_exp_f32 v53, v53\n" \
  "v_exp_f32 v54, v54\n" \
  "v_exp_f32 v55, v55\n" \
  "v_exp_f32 v24, v24\n" \
  "v_exp_f32 v25, v25\n" \
  "v_smfmac_f32_32x32x32_bf16 v[86:101], v[114:117], v[48:55], v6\n" \
  "v_exp_f32 v26, v26\n" \
  "v_exp_f32 v27, v27\n" \
  "v_exp_f32 v28, v28\n" \
  "v_mfma_f32_32x32x16_bf16 v[40:55], v[106:109], v[12:15], 0\n" \
  "v_exp_f32 v29, v29\n" \
  "buffer_load_dwordx4 v[102:105], v4, %[rs], %[so] offen sc1\n s_mov_b64 exec, %[vm]\n buffer_load_dwordx4 v[110:113], v5, %[rs], %[so] offen sc1\n buffer_load_dwordx4 v[114:117], v5, %[rs], %[so] offen offset:512 sc1\n s_mov_b64 exec, -1\n" \
  "v_exp_f32 v30, v30\n" \
  "s_add_u32 %[ti], %[ti], %[st]\n" \
  "v_exp_f32 v31, v31\n" \
  "s_and_b32 %[ti], %[ti], 0x1ffff\n" \
  "v_exp_f32 v32, v32\n" \
  "s_add_u32 %[so], %[ti], %[hb]\n" \
  "v_exp_f32 v33, v33\n" \
  "v_exp_f32 v34, v34\n" \
  "v_smfmac_f32_32x32x32_bf16 v[56:71], v[118:121], v[24:31], v6\n" \
  "v_exp_f32 v35, v35\n" \
  "v_exp_f32 v36, v36\n" \
  "v_exp_f32 v37, v37\n" \
  "v_exp_f32 v38, v38\n" \
  "v_exp_f32 v39, v39\n" \
  "v_exp_f32 v40, v40\n" \
  "v_exp_f32 v41, v41\n" \
  "v_smfmac_f32_32x32x32_bf16 v[56:71], v[122:125], v[32:39], v6\n" \
  "v_exp_f32 v42, v42\n" \
  "v_exp_f32 v43, v43\n" \
  "v_exp_f32 v44, v44\n" \
  "v_mfma_f32_32x32x16_bf16 v[24:39], v[106:109], v[16:19], 0\n" \
  "v_exp_f32 v45, v45\n" \
  "buffer_load_dwordx4 %[w18], v4, %[rs], %[pw] offen offset:2048 sc1\n buffer_load_dwordx4 %[w19], v4, %[rs], %[pw] offen offset:3072 sc1\n s_add_u32 %[pw], %[pw], 0x1000\n buffer_load_dwordx4 %[w20], v4, %[rs], %[pw] offen offset:0 sc1\n" \
  "v_exp_f32 v46, v46\n" \
  "v_exp_f32 v47, v47\n" \
  "v_exp_f32 v48, v48\n" \
  "v_exp_f32 v49, v49\n" \
  "v_exp_f32 v50, v50\n" \
  "v_smfmac_f32_32x32x32_bf16 v[66:81], v[118:121], v[40:47], v6\n" \
  "v_exp_f32 v51, v51\n" \
  "v_exp_f32 v52, v52\n" \
  "v_exp_f32 v53, v53\n" \
  "v_exp_f32 v54, v54\n" \
  "v_exp_f32 v55, v55\n" \
  "v_exp_f32 v24, v24\n" \
  "v_exp_f32 v25, v25\n" \
  "v_smfmac_f32_32x32x32_bf16 v[66:81], v[122:125], v[48:55], v6\n" \
  "v_exp_f32 v26, v26\n" \
  "v_exp_f32 v27, v27\n" \
  "v_exp_f32 v28, v28\n" \
  "v_mfma_f32_32x32x16_bf16 v[40:55], v[106:109], v[20:23], 0\n" \
  "v_exp_f32 v29, v29\n" \
  "buffer_load_dwordx4 %[w21], v4, %[rs], %[pw] offen offset:1024 sc1\n buffer_load_dwordx4 %[w22], v4, %[rs], %[pw] offen offset:2048 sc1\n buffer_load_dwordx4 %[w23], v4, %[rs], %[pw] offen offset:3072 sc1\n s_add_u32 %[pw], %[pw], 0x1000\n" \
  "v_exp_f32 v30, v30\n" \
  "v_exp_f32 v31, v31\n" \
  "v_exp_f32 v32, v32\n" \
  "v_exp_f32 v33, v33\n" \
  "v_exp_f32 v34, v34\n" \
  "v_smfmac_f32_32x32x32_bf16 v[76:91], v[118:121], v[24:31], v6\n" \
  "v_exp_f32 v35, v35\n" \
  "v_exp_f32 v36, v36\n" \
  "v_exp_f32 v37, v37\n" \
  "v_exp_f32 v38, v38\n" \
  "v_exp_f32 v39, v39\n" \
  "v_exp_f32 v40, v40\n" \
  "v_exp_f32 v41, v41\n" \
  "v_smfmac_f32_32x32x32_bf16 v[76:91], v[122:125], v[32:39], v6\n" \
  "v_exp_f32 v42, v42\n" \
  "v_exp_f32 v43, v43\n" \
  "v_exp_f32 v44, v44\n" \
  "s_waitcnt vmcnt(6)\n" \
  "v_mfma_f32_32x32x16_bf16 v[24:39], v[102:105], v[8:11], 0\n" \
  "v_exp_f32 v45, v45\n" \
  "v_exp_f32 v46, v46\n" \
  "v_exp_f32 v47, v47\n" \
  "v_exp_f32 v48, v48\n" \
  "v_exp_f32 v49, v49\n" \
  "v_exp_f32 v50, v50\n" \
  "v_smfmac_f32_32x32x32_bf16 v[86:101], v[118:121], v[40:47], v6\n" \
  "v_exp_f32 v51, v51\n" \
  "v_exp_f32 v52, v52\n" \
  "v_exp_f32 v53, v53\n" \
  "v_exp_f32 v54, v54\n" \
  "v_exp_f32 v55, v55\n" \
  "v_exp_f32 v24, v24\n" \
  "v_exp_f32 v25, v25\n" \
  "v_smfmac_f32_32x32x32_bf16 v[86:101], v[122:125], v[48:55], v6\n" \
  "v_exp_f32 v26, v26\n" \
  "v_exp_f32 v27, v27\n" \
  "v_exp_f32 v28, v28\n" \
  "v_mfma_f32_32x32x16_bf16 v[40:55], v[102:105], v[12:15], 0\n" \
  "v_exp_f32 v29, v29\n" \
  "s_mov_b32 m0, %[pm]\n" \
  "v_exp_f32 v30, v30\n" \
  "s_nop 0\n" \
  "v_exp_f32 v31, v31\n" \
  "buffer_load_dwordx4 v4, %[rs], %[ps] offen offset:0 sc1 lds\n" \
  "v_exp_f32 v32, v32\n" \
  "buffer_load_dwordx4 v4, %[rs], %[ps] offen offset:1024 sc1 lds\n" \
  "v_exp_f32 v33, v33\n" \
  "buffer_load_dwordx4 v4, %[rs], %[ps] offen offset:2048 sc1 lds\n" \
  "v_exp_f32 v34, v34\n" \
  "v_smfmac_f32_32x32x32_bf16 v[56:71], v[110:113], v[24:31], v6\n" \
  "v_exp_f32 v35, v35\n" \
  "v_exp_f32 v36, v36\n" \
  "v_exp_f32 v37, v37\n" \
  "v_exp_f32 v38, v38\n" \
  "v_exp_f32 v39, v39\n" \
  "v_exp_f32 v40, v40\n" \
  "v_exp_f32 v41, v41\n" \
  "v_smfmac_f32_32x32x32_bf16 v[56:71], v[114:117], v[32:39], v6\n" \
  "v_exp_f32 v42, v42\n" \
  "v_exp_f32 v43, v43\n" \
  "v_exp_f32 v44, v44\n" \
  "v_mfma_f32_32x32x16_bf16 v[24:39], v[102:105], v[16:19], 0\n" \
  "v_exp_f32 v45, v45\n" \
  "buffer_load_dwordx4 v4, %[rs], %[ps] offen offset:3072 sc1 lds\n" \
  "v_exp_f32 v46, v46\n" \
  "s_add_u32 %[pm], %[pm], 0x1000\n" \
  "v_exp_f32 v47, v47\n" \
  "s_add_u32 %[ps], %[ps], 0x1000\n" \
  "v_exp_f32 v48, v48\n" \
  "s_mov_b32 m0, %[pm]\n" \
  "v_exp_f32 v49, v49\n" \
  "s_nop 0\n" \
  "v_exp_f32 v50, v50\n" \
  "v_smfmac_f32_32x32x32_bf16 v[66:81], v[110:113], v[40:47], v6\n" \
  "v_exp_f32 v51, v51\n" \
  "v_exp_f32 v52, v52\n" \
  "v_exp_f32 v53, v53\n" \
  "v_exp_f32 v54, v54\n" \
  "v_exp_f32 v55, v55\n" \
  "v_exp_f32 v24, v24\n" \
  "v_exp_f32 v25, v25\n" \
  "v_smfmac_f32_32x32x32_bf16 v[66:81], v[114:117], v[48:55], v6\n" \
  "v_exp_f32 v26, v26\n" \
  "v_exp_f32 v27, v27\n" \
  "v_exp_f32 v28, v28\n" \
  "v_mfma_f32_32x32x16_bf16 v[40:55], v[102:105], v[20:23], 0\n" \
  "v_exp_f32 v29, v29\n" \
  "buffer_load_dwordx4 v4, %[rs], %[ps] offen offset:0 sc1 lds\n" \
  "v_exp_f32 v30, v30\n" \
  "buffer_load_dwordx4 v4, %[rs], %[ps] offen offset:1024 sc1 lds\n" \
  "v_exp_f32 v31, v31\n" \
  "buffer_load_dwordx4 v4, %[rs], %[ps] offen offset:2048 sc1 lds\n" \
  "v_exp_f32 v32, v32\n" \
  "buffer_load_dwordx4 v4, %[rs], %[ps] offen offset:3072 sc1 lds\n" \
  "v_exp_f32 v33, v33\n" \
  "v_exp_f32 v34, v34\n" \
  "v_smfmac_f32_32x32x32_bf16 v[76:91], v[110:113], v[24:31], v6\n" \
  "v_exp_f32 v35, v35\n" \
  "v_exp_f32 v36, v36\n" \
  "v_exp_f32 v37, v37\n" \
  "v_exp_f32 v38, v38\n" \
  "v_exp_f32 v39, v39\n" \
  "v_exp_f32 v40, v40\n" \
  "v_exp_f32 v41, v41\n" \
  "v_smfmac_f32_32x32x32_bf16 v[76:91], v[114:117], v[32:39], v6\n" \
  "v_exp_f32 v42, v42\n" \
  "v_exp_f32 v43, v43\n" \
  "v_exp_f32 v44, v44\n" \
  "s_nop 0\n" \
  "v_exp_f32 v45, v45\n" \
  "v_exp_f32 v46, v46\n" \
  "v_exp_f32 v47, v47\n" \
  "v_exp_f32 v48, v48\n" \
  "v_exp_f32 v49, v49\n" \
  "v_exp_f32 v50, v50\n" \
  "v_smfmac_f32_32x32x32_bf16 v[86:101], v[110:113], v[40:47], v6\n" \
  "v_exp_f32 v51, v51\n" \
  "v_exp_f32 v52, v52\n" \
  "v_exp_f32 v53, v53\n" \
  "v_exp_f32 v54, v54\n" \
  "v_exp_f32 v55, v55\n" \
  "s_nop 1\n" \
  "v_smfmac_f32_32x32x32_bf16 v[86:101], v[114:117], v[48:55], v6\n" \
  "s_nop 11\n" \
  "s_waitcnt vmcnt(8)\n" \
  ""
#define ATTN_CLOB "v6", "v24", "v25", "v26", "v27", "v28", "v29", "v30", "v31", "v32", "v33", "v34", "v35", "v36", "v37", "v38", "v39", "v40", "v41", "v42", "v43", "v44", "v45", "v46", "v47", "v48", "v49", "v50", "v51", "v52", "v53", "v54", "v55", "v65", "v75", "v85", "v95", "v96", "v97", "v98", "v99", "v100", "v101", "v102", "v103", "v104", "v105", "v106", "v107", "v108", "v109"
#define ATTN_NITER 5
#define ATTN_STATE_OUT(o, l, vr) "=&{v[56:63]}"(o[0]), "=&{v64}"(l[0]), "=&{v[66:73]}"(o[1]), "=&{v74}"(l[1]), "=&{v[76:83]}"(o[2]), "=&{v84}"(l[2]), "=&{v[86:93]}"(o[3]), "=&{v94}"(l[3]), "=&{v[110:117]}"(vr[0]), "=&{v[118:125]}"(vr[1])
#define ATTN_STATE(o, l, vr) "+{v[56:63]}"(o[0]), "+{v64}"(l[0]), "+{v[66:73]}"(o[1]), "+{v74}"(l[1]), "+{v[76:83]}"(o[2]), "+{v84}"(l[2]), "+{v[86:93]}"(o[3]), "+{v94}"(l[3]), "+{v[110:117]}"(vr[0]), "+{v[118:125]}"(vr[1])
#define ATTN_OUT_V(vr) "=&{v[110:117]}"(vr[0]), "=&{v[118:125]}"(vr[1])
#define ATTN_OUT_A(k) "={v[56:63]}"(k.o[0]), "={v64}"(k.l0), "={v[66:73]}"(k.o[1]), "={v74}"(k.l1)
#define ATTN_OUT_B(k) "={v[76:83]}"(k.o[2]), "={v84}"(k.l2), "={v[86:93]}"(k.o[3]), "={v94}"(k.l3)
#define ATTN_WD(wd) "+{v[126:129]}"(wd[0]), "+{v[130:133]}"(wd[1]), "+{v[134:137]}"(wd[2]), "+{v[138:141]}"(wd[3]), "=&{v[142:145]}"(wd[4]), "=&{v[146:149]}"(wd[5]), [w6] "=&v"(wd[6]), [w7] "=&v"(wd[7]), [w8] "=&v"(wd[8]), [w9] "=&v"(wd[9]), [w10] "=&v"(wd[10]), [w11] "=&v"(wd[11]), [w12] "=&v"(wd[12]), [w13] "=&v"(wd[13]), [w14] "=&v"(wd[14]), [w15] "=&v"(wd[15]), [w16] "=&v"(wd[16]), [w17] "=&v"(wd[17]), [w18] "=&v"(wd[18]), [w19] "=&v"(wd[19]), [w20] "=&v"(wd[20]), [w21] "=&v"(wd[21]), [w22] "=&v"(wd[22]), [w23] "=&v"(wd[23])
#define ATTN_Q(q) "{v[8:11]}"(q[0]), "{v[12:15]}"(q[1]), "{v[16:19]}"(q[2]), "{v[20:23]}"(q[3])

namespace {
constexpr int SEQ = 2048, DM = 512, FF = 1792;
constexpr int NWG = 256, NTHR = 512;
constexpr int KB = 1024, MB = 1024 * 1024;
constexpr int W_FLAG = 816 * KB, W_START = 0, W_EPOCH = 56 * KB;
constexpr int W_CSG = 704 * KB, CSG_BYTES = 14 * KB;
constexpr int W_S0 = 2 * MB + 512 * KB;
constexpr int W_ST = 128 * KB;
constexpr int ST_BYTES = 128 * KB;
constexpr int W_WQ = 1 * MB;
constexpr int W_WO = 3 * MB;
constexpr int W_WG = 4 * MB;
constexpr int W_WD = 8 * MB;
constexpr int W_X = 10 * MB;
constexpr int W_OF = 18 * MB;
constexpr int W_KV = 22 * MB;
constexpr int W_HF = 30 * MB;
constexpr int W_END = 46 * MB;
enum { F_Q = 0, F_G = 1, F_C = 2 };
typedef unsigned long long u64;
constexpr unsigned SPIN_CAP = 1u << 17;
constexpr int L_A0 = 0, L_A1 = 32 * KB, L_A2 = 64 * KB, L_A3 = 96 * KB, L_WO = 80 * KB, L_OX = 32 * KB;
constexpr int L_GX = 128 * KB, L_KX = 0;
constexpr int L_MISC = 144 * KB;
constexpr int L_QX = L_MISC;
constexpr int L_MS = L_MISC + 8 * KB;
constexpr int L_NR = L_MISC + 10 * KB;
constexpr int L_SP = L_MISC + 11 * KB;
constexpr int L_DEC = L_MISC + 13 * KB;
constexpr int L_CS = L_MISC + 14 * KB;
constexpr int LDS_BYTES = 160 * KB;
constexpr float QSCALE = 0.25f * 1.4426950408889634f;
constexpr float EPS = 1e-5f;
constexpr int SC1 = 16;

struct Params {
  const unsigned short *x, *wqkv, *wo, *wg, *wu, *wd;
  unsigned short* out;
  unsigned char* ws;
};

DI f32x16 mfma(v4u a, v4u b, f32x16 c) {
  return __builtin_amdgcn_mfma_f32_32x32x16_bf16(__builtin_bit_cast(bf16x8, a), __builtin_bit_cast(bf16x8, b), c, 0, 0, 0);
}
DI f32x4 mfma16(v4u a, v4u b, f32x4 c) {
  return __builtin_amdgcn_mfma_f32_16x16x32_bf16(__builtin_bit_cast(bf16x8, a), __builtin_bit_cast(bf16x8, b), c, 0, 0, 0);
}
DI unsigned pk(float lo, float hi) {
  bf16x2 v = {(__bf16)lo, (__bf16)hi};
  return __builtin_bit_cast(unsigned, v);
}
DI float u2f(unsigned u) { return __builtin_bit_cast(float, u); }
DI unsigned f2u(float f) { return __builtin_bit_cast(unsigned, f); }
DI bool wave_all(bool p) { return __builtin_amdgcn_ballot_w64(!p) == 0; }
DI float blo(unsigned u) { return __builtin_bit_cast(float, u << 16); }
DI float bhi(unsigned u) { return __builtin_bit_cast(float, u & 0xffff0000u); }
DI v4u pk8(f32x4 a, f32x4 b) { return v4u{pk(a[0], a[1]), pk(a[2], a[3]), pk(b[0], b[1]), pk(b[2], b[3])}; }
DI f32x4 up_lo(v4u v) { return f32x4{blo(v[0]), bhi(v[0]), blo(v[1]), bhi(v[1])}; }
DI f32x4 up_hi(v4u v) { return f32x4{blo(v[2]), bhi(v[2]), blo(v[3]), bhi(v[3])}; }
DI v4u ld_sc1(rsrc_t rs, int voff, int soff) { return __builtin_amdgcn_raw_buffer_load_b128(rs, voff, soff, SC1); }
DI v4u ld_pl(rsrc_t rs, int voff, int soff) { return __builtin_amdgcn_raw_buffer_load_b128(rs, voff, soff, 0); }
DI void st_sc1(v4u v, rsrc_t rs, int voff, int soff) {
  __builtin_amdgcn_raw_buffer_store_b128(v, rs, voff, soff, SC1);
  asm volatile("s_nop 1" :: "v"(v) : "memory");
}
DI v4u lds_r(lptr p) { return *(LAS v4u*)p; }
DI void lds_w(lptr p, v4u v) { *(LAS v4u*)p = v; }
DI f32x4 lds_rf(lptr p) { return *(LAS f32x4*)p; }
DI void lds_wf(lptr p, f32x4 v) { *(LAS f32x4*)p = v; }
#define BAR() asm volatile("s_waitcnt lgkmcnt(0)\n\ts_barrier" ::: "memory")
#define DRAIN() asm volatile("s_waitcnt vmcnt(0)" ::: "memory")
#define WAITV(n) asm volatile("s_waitcnt vmcnt(" #n ")" ::: "memory")
#define SBAR() __builtin_amdgcn_sched_barrier(0)
#define PIN4(a, i) asm volatile("" : "+v"(a[i]), "+v"(a[(i) + 1]), "+v"(a[(i) + 2]), "+v"(a[(i) + 3]))
template <bool COH> DI void dma4(rsrc_t rs, unsigned lds, unsigned voff, unsigned soff) {
  lds = __builtin_amdgcn_readfirstlane(lds);
  soff = __builtin_amdgcn_readfirstlane(soff);
  if constexpr (COH)
    asm volatile("s_mov_b32 m0, %0\n\ts_nop 3\n\tbuffer_load_dwordx4 %1, %2, %3 offen sc1 lds\n\tbuffer_load_dwordx4 %1, %2, %3 offen offset:1024 sc1 lds\n\t"
                 "buffer_load_dwordx4 %1, %2, %3 offen offset:2048 sc1 lds\n\tbuffer_load_dwordx4 %1, %2, %3 offen offset:3072 sc1 lds"
                 :: "s"(lds), "v"(voff), "s"(rs), "s"(soff) : "memory");
  else
    asm volatile("s_mov_b32 m0, %0\n\ts_nop 3\n\tbuffer_load_dwordx4 %1, %2, %3 offen lds\n\tbuffer_load_dwordx4 %1, %2, %3 offen offset:1024 lds\n\t"
                 "buffer_load_dwordx4 %1, %2, %3 offen offset:2048 lds\n\tbuffer_load_dwordx4 %1, %2, %3 offen offset:3072 lds"
                 :: "s"(lds), "v"(voff), "s"(rs), "s"(soff) : "memory");
}
DI void dma1p(rsrc_t rs, unsigned lds, unsigned voff, unsigned soff) {
  lds = __builtin_amdgcn_readfirstlane(lds);
  soff = __builtin_amdgcn_readfirstlane(soff);
  asm volatile("s_mov_b32 m0, %0\n\ts_nop 3\n\tbuffer_load_dwordx4 %1, %2, %3 offen lds" :: "s"(lds), "v"(voff), "s"(rs), "s"(soff) : "memory");
}
template <int OFF> DI void dma1(rsrc_t rs, unsigned lds, unsigned voff, unsigned soff) {
  asm volatile("s_mov_b32 m0, %0\n\ts_nop 0\n\tbuffer_load_dwordx4 %1, %2, %3 offen offset:%4 sc1 lds" :: "s"(lds), "v"(voff), "s"(rs), "s"(soff), "i"(OFF) : "memory");
}
DI lptr opaque(lptr p) {
  asm volatile("" : "+v"(p));
  return p;
}
DI float xhalf_add(float v) {
  auto r = __builtin_amdgcn_permlane32_swap(__builtin_bit_cast(unsigned, v), __builtin_bit_cast(unsigned, v), false, false);
  return u2f(r[0]) + u2f(r[1]);
}
DI float xhalf_max(float v) {
  auto r = __builtin_amdgcn_permlane32_swap(__builtin_bit_cast(unsigned, v), __builtin_bit_cast(unsigned, v), false, false);
  return __builtin_fmaxf(u2f(r[0]), u2f(r[1]));
}
DI float xrow_add(float v) {
  auto r = __builtin_amdgcn_permlane16_swap(__builtin_bit_cast(unsigned, v), __builtin_bit_cast(unsigned, v), false, false);
  return u2f(r[0]) + u2f(r[1]);
}
DI v4u pair_rows(unsigned a0, unsigned a1, unsigned b0, unsigned b1) {
  auto x = __builtin_amdgcn_permlane16_swap(a0, b0, false, false);
  auto y = __builtin_amdgcn_permlane16_swap(a1, b1, false, false);
  return v4u{x[0], y[0], x[1], y[1]};
}
DI v4u lane_t8(v4u v, int lane) {
  const int src = (((lane & 7) << 3) | (lane >> 3)) * 4;
  return v4u{(unsigned)__builtin_amdgcn_ds_bpermute(src, (int)v[0]), (unsigned)__builtin_amdgcn_ds_bpermute(src, (int)v[1]),
             (unsigned)__builtin_amdgcn_ds_bpermute(src, (int)v[2]), (unsigned)__builtin_amdgcn_ds_bpermute(src, (int)v[3])};
}
template <int CTRL> DI float dpp_add(float v) {
  return v + __builtin_bit_cast(float, __builtin_amdgcn_update_dpp(0, __builtin_bit_cast(int, v), CTRL, 0xf, 0xf, true));
}
template <int CTRL> DI unsigned dpp_max(unsigned v) {
  unsigned o = (unsigned)__builtin_amdgcn_update_dpp(0, (int)v, CTRL, 0xf, 0xf, true);
  return v > o ? v : o;
}
DI unsigned row16_max(unsigned v) {
  v = dpp_max<0xb1>(v);
  v = dpp_max<0x4e>(v);
  v = dpp_max<0x141>(v);
  v = dpp_max<0x140>(v);
  return v;
}
DI unsigned umax(unsigned a, unsigned b) { return a > b ? a : b; }
DI unsigned fabs_bits(float f) { return __builtin_bit_cast(unsigned, f) & 0x7fffffffu; }

DI int flag_off(int kind, int layer, int line) { return W_FLAG + ((kind * 2 + layer) * 16 + line) * 256; }
DI GAS u64* flag_word(unsigned char* ws, int kind, int layer, int line, int idx) { return (GAS u64*)(ws + flag_off(kind, layer, line) + idx * 8); }
DI bool flag_ge(u64 v, u64 tag, unsigned want) { return v - tag - want <= 0xffffu - want; }
DI void poll16(unsigned char* ws, u64 tag, int kind, int layer, int line, unsigned want, int w, int lane) {
  if (w == 0) {
    GAS u64* f = flag_word(ws, kind, layer, line, lane & 15);
    for (unsigned spins = 0; spins < SPIN_CAP; ++spins) {
      const u64 v = __hip_atomic_load(f, RLX_AGENT);
      if (wave_all(flag_ge(v, tag, want))) break;
    }
    __builtin_amdgcn_fence(__ATOMIC_ACQUIRE, "wavefront");
  }
}
DI void post(unsigned char* ws, u64 tag, int kind, int layer, int line, int idx, unsigned val) {
  __hip_atomic_store(flag_word(ws, kind, layer, line, idx), tag | val, RLX_AGENT);
}
DI void row_stats_(v4u a, float& mean, float& rstd) {
  const f32x4 p = __builtin_bit_cast(f32x4, a);
  const float s = xhalf_add(xrow_add(p[0] + p[2])), ss = xhalf_add(xrow_add(p[1] + p[3]));
  mean = s * (1.f / DM);
  const float var = __builtin_fmaxf(ss * (1.f / DM) - mean * mean, 0.f);
  rstd = __builtin_amdgcn_rsqf(var + EPS);
}

constexpr unsigned EMPTY = 0xffffffffu;
DI unsigned umin(unsigned a, unsigned b) { return a < b ? a : b; }
DI v4u no_empty(v4u v) { return v4u{umin(v[0], EMPTY - 1), umin(v[1], EMPTY - 1), umin(v[2], EMPTY - 1), umin(v[3], EMPTY - 1)}; }
DI unsigned seen(unsigned m, v4u v) { return umax(umax(umax(umax(m, v[0]), v[1]), v[2]), v[3]); }
DI void poll_data(rsrc_t rs, int voff, int soff) {
  for (unsigned spins = 0; spins < SPIN_CAP; ++spins) {
    const unsigned v = __builtin_amdgcn_raw_buffer_load_b32(rs, voff, soff, SC1);
    asm volatile("" ::: "memory");
    if (wave_all(v != EMPTY)) break;
  }
}
DI void row_stats(rsrc_t rs, v4u a, int voff, int soff, float& mean, float& rstd) {
  if (__builtin_expect(!wave_all(seen(0, a) != EMPTY), 0)) {
    for (unsigned spins = 0; spins < SPIN_CAP; ++spins) {
      a = ld_sc1(rs, voff, soff);
      asm volatile("" ::: "memory");
      if (wave_all(seen(0, a) != EMPTY)) break;
    }
  }
  row_stats_(a, mean, rstd);
}
DI int x_probe(int lane) { return (4 * (lane & 3) + 3) * KB + (32 + 8 * ((lane >> 2) & 3) + 7) * 16 + 12; }

#define ABL 0
#define STAMP(i)
#define STAMP2(i)
struct Ctx {
  rsrc_t rs;
  unsigned char* ws;
  lptr lds;
  unsigned ldsb;
  u64 tag;
  int rb, ci;
  int ro, io, cb2, rb2;
  int w, lane, r, h, tid;
};

DI Ctx fresh(const Ctx& c0) {
  Ctx c = c0;
  asm volatile("" : "+v"(c.lane));
  asm volatile("" : "+s"(c.w));
  asm volatile("" : "+s"(c.rb));
  asm volatile("" : "+s"(c.ci));
  asm volatile("" : "+s"(c.ro));
  asm volatile("" : "+s"(c.io));
  asm volatile("" : "+s"(c.tag));
  asm volatile("" : "+s"(c.ws));
  c.tid = c.w * 64 + c.lane;
  c.r = c.lane & 31;
  c.h = c.lane >> 5;
  c.cb2 = c.io & 7;
  c.rb2 = 2 * c.ro + (c.io >> 3);
  __builtin_assume(c.lane >= 0 && c.lane < 64);
  __builtin_assume(c.w >= 0 && c.w < 8);
  return c;
}

typedef short v4s __attribute__((ext_vector_type(4)));
DI v2u lds_tr(lptr p) { return __builtin_bit_cast(v2u, __builtin_amdgcn_ds_read_tr16_b64_v4i16((LAS v4s*)p)); }
DI int tile_off(int r, int c32) { return r * 256 + ((c32 ^ (((r & 3) << 1) | ((r >> 3) & 1))) << 5); }

DI int xfrag_voff(const Ctx& c) { return (c.lane >> 4) * KB + (((c.lane >> 3) & 1) * 32 + (c.w & 3) * 8 + (c.lane & 7)) * 16; }
DI int xfrag_soff(const Ctx& c, int xi) { return W_X + xi * 2 * MB + ((2 * c.rb2 + (c.w >> 2)) * 32 + 4 * c.cb2) * KB; }
DI int st_soff(const Ctx& c, int xi) { return W_ST + xi * ST_BYTES + (2 * c.rb2 + (c.w >> 2)) * 2 * KB + (c.cb2 >> 1) * 512 + (c.w & 3) * 128 + (c.cb2 & 1) * 8; }
DI int st_voff(const Ctx& c) { return c.lane < 8 ? c.lane * 16 : W_END; }
DI int of_soff(const Ctx& c, int L) { return W_OF + L * 2 * MB + ((4 * c.rb + (c.w >> 1)) * 32 + 2 * c.ci + (c.w & 1)) * KB; }

struct Tile { const unsigned short* src; int ld, kind, d0, d1, d2, dcs; };
DI Tile tile_task(const Params& p, const Ctx& c, int b) {
  const int w = c.w;
  const int kind = (b >= 48) + (b >= 64) + (b >= 176) + (b >= 232);
  Tile t;
  t.dcs = W_END;
  if (kind == 2) {
    const int kb = (b - 64) / 14, cbk = (b - 64) % 14, col = 8 * cbk + w;
    t.ld = FF;
    t.src = (w < 4 ? p.wg : p.wu) + (64 * kb + 16 * (w & 3)) * FF + 128 * cbk;
    t.d0 = W_WG + (col * 32 + 4 * kb) * KB; t.d1 = 2 * KB; t.d2 = KB;
    t.dcs = W_CSG + kb * CSG_BYTES + col * 128;
  } else if (kind & 1) {
    const int kb = (b - (kind == 1 ? 48 : 176)) >> 2, cbk = b & 3, U = 8 * kb + w, j = U / 7;
    t.ld = DM;
    t.src = (kind == 1 ? p.wo : p.wd) + (128 * kb + 16 * w) * DM + 128 * cbk;
    t.d0 = kind == 1 ? W_WO + (4 * cbk * 32 + U) * KB : W_WD + (4 * cbk * 112 + 28 * (j >> 2) + 4 * (U - 7 * j) + (j & 3)) * KB;
    t.d1 = (kind == 1 ? 32 : 112) * KB; t.d2 = 2 * t.d1;
  } else {
    const int bb = kind ? 0 : b, kb = bb / 12, cbk = bb % 12, col = 8 * cbk + w, mat = col >> 5, head = col & 31;
    t.ld = 3 * DM;
    t.src = p.wqkv + (128 * kb + 16 * w) * 3 * DM + 128 * cbk;
    t.d0 = kind ? W_END : W_WQ + (((head >> 1) * 16 + 4 * kb) * 6 + (mat == 2 ? 4 + (head & 1) : 2 * (head & 1) + mat)) * KB;
    t.d1 = 6 * KB; t.d2 = 12 * KB;
  }
  t.kind = kind;
  asm volatile("" : "+s"(t.src));
  asm volatile("" : "+s"(t.ld));
  asm volatile("" : "+s"(t.kind));
  asm volatile("" : "+s"(t.d0));
  asm volatile("" : "+s"(t.d1));
  asm volatile("" : "+s"(t.d2));
  asm volatile("" : "+s"(t.dcs));
  return t;
}
DI void tile_load(const Ctx& c, const Tile& tt, v4u (&raw)[4]) {
  const rsrc_t rt = __builtin_amdgcn_make_buffer_rsrc((void*)tt.src, 0, 16 * FF * 2, 0x00020000);
  const int vo = ((c.lane >> 4) * tt.ld + 8 * (c.lane & 15)) * 2;
#pragma unroll
  for (int e = 0; e < 4; ++e) raw[e] = ld_pl(rt, vo, e * 8 * tt.ld);
}
DI void tile_put(const Ctx& c, lptr T, const v4u (&raw)[4]) {
#pragma unroll
  for (int e = 0; e < 4; ++e) lds_w(T + tile_off(16 * c.w + 4 * e + (c.lane >> 4), (c.lane & 15) >> 1) + (c.lane & 1) * 16, raw[e]);
}
DI void tile_read(const Ctx& c, const Tile& tt, lptr T, v4u (&fr)[4]) {
  const int g = c.lane >> 4, q4 = (c.lane & 15) >> 2, p4 = c.lane & 3, kind = tt.kind;
  const int wide = kind & 1, k1 = wide & ~(kind >> 1);
  const int r0 = q4 + wide * (16 * c.w + (8 - 4 * k1) * c.h) + (1 - wide) * 8 * g, r1 = r0 + 4 + 4 * k1;
  const int c0 = wide * (g & 1) + (1 - wide) * c.w, er = (1 - wide) * 32 * 256, ec = wide * 64;
  const int o0 = tile_off(r0, c0) + p4 * 8, o1 = tile_off(r1, c0) + p4 * 8;
#pragma unroll
  for (int e = 0; e < 4; ++e) {
    const v2u a = lds_tr(T + ((o0 + e * er) ^ (e * ec))), d = lds_tr(T + ((o1 + e * er) ^ (e * ec)));
    fr[e] = v4u{a[0], a[1], d[0], d[1]};
  }
}
DI int tile_dst(const Tile& tt, int e) { return __builtin_amdgcn_readfirstlane(tt.d0 + (e & 1) * tt.d1 + (e >> 1) * tt.d2); }
template <int N> DI unsigned tile_colsum(const Ctx& c, const v4u (&fr)[N]) {
  float cs[2] = {0.f, 0.f};
#pragma unroll
  for (int e = 0; e < 4; ++e)
    cs[e >> 1] += (blo(fr[e][0]) + bhi(fr[e][0])) + (blo(fr[e][1]) + bhi(fr[e][1])) + (blo(fr[e][2]) + bhi(fr[e][2])) + (blo(fr[e][3]) + bhi(fr[e][3]));
  cs[0] = xhalf_add(xrow_add(cs[0]));
  cs[1] = xhalf_add(xrow_add(cs[1]));
  return f2u((c.lane & 16) ? cs[1] : cs[0]);
}

DI void early_reads(const Params& p, const Ctx& c) {
  if (c.w == 7) {
    const int k = c.lane - 48;
    const unsigned char* q = c.lane < 46 ? p.ws + c.lane * MB : c.lane < 48 ? (const unsigned char*)p.out + (c.lane & 1) * MB
                           : k < 6 ? p.ws + flag_off(k >> 1, k & 1, (k >> 1) == F_G ? c.rb : c.ci)
                           : k < 8 ? p.ws + W_START + BID * 8
                           : (const unsigned char*)(k < 10 ? p.wo : k < 12 ? p.wg : k < 14 ? p.wu : p.wd) + (k & 1) * (k < 10 ? 0 : MB);
    asm volatile("s_mov_b32 m0, %0\n\ts_nop 0\n\tglobal_load_lds_dword %1, off sc1" :: "s"(c.ldsb + L_SP), "v"(q) : "memory");
  }
}
struct Chk { v4u qf, kf; f32x4 v[2]; f32x8 o[4], vr[2]; float l0, l1, l2, l3; };
struct Cs { f32x4 q, k; float v0, v1; };
DI Cs qkv_colsums(const Ctx& c) {
  const int kh = c.w & 1, t = c.lane & 15, g = c.lane >> 4;
  return Cs{lds_rf(c.lds + L_CS + kh * 128 + g * 16), lds_rf(c.lds + L_CS + kh * 128 + 64 + g * 16), *(LAS float*)(c.lds + L_CS + 256 + t * 4),
            *(LAS float*)(c.lds + L_CS + 320 + t * 4)};
}
template <int L> DI void phase_qkv(const Params& p, const Ctx& c, v4u& res, v4u (&raw)[4], Tile& tt, Chk& ck_, Cs cs, u64& epoch) {
  const int tb = c.w >> 1, kh = c.w & 1, TB = 4 * c.rb + tb, t = c.lane & 15, g = c.lane >> 4;
  const rsrc_t rx = L ? c.rs : __builtin_amdgcn_make_buffer_rsrc((void*)p.x, 0, SEQ * DM * 2, 0x00020000);
  const int xoff = L ? W_X + 2 * 2 * MB + (TB * 32 + 16 * kh) * KB : TB * 32 * KB + kh * 512;
  const int av = (g >> 1) * KB + ((g & 1) * 32 + t) * 16, av0 = av + kh * 256, av1 = av + (1 - kh) * 256;
  const int rot = (c.ci + c.rb) & 3;
  v4u act[8][2];
#define QA1(s, v) ld_sc1(rx, (v) + ((s) & 1) * 2 * KB, xoff + ((s) >> 1) * 4 * KB)
#define QA(s) { act[s][0] = QA1(s, av0); act[s][1] = QA1(s, av1); }
  const int soff = W_ST + 2 * ST_BYTES + TB * 32 * 64, sv0 = (t + 16 * kh) * 16 + g * 512, sv1 = (t + 16 * (1 - kh)) * 16 + g * 512;
  f32x4 acc[6][2];
  constexpr int P1[4] = {1, 3, 4, 5};
  const lptr wb = c.lds + L_A0 + kh * 48 * KB;
  const lptr wv = wb + c.lane * 16, wo = wv + kh * 2 * KB, wx = wv + (1 - kh) * 2 * KB;
  const int trb = (8 * g + (t >> 2)) * 64 + (t & 3) * 8;
  const lptr to = wb + trb + ((kh ^ g) & 1) * 32, tx = wb + trb + ((kh ^ g ^ 1) & 1) * 32, t0 = wb + trb + (g & 1) * 32, t1 = wb + trb + ((g ^ 1) & 1) * 32;
  f32x4 cf;
  v4u cfa, cfb;
  auto qw = [&](int s, int i, int off = 0) __attribute__((always_inline)) NOPK -> v4u {
    if (L) return lds_r((i < 2 ? wo + i * KB : i < 4 ? wx + (i - 2) * KB : wv + i * KB) + s * 6 * KB);
    const lptr q = (i < 2 ? to : i < 4 ? tx : i == 4 ? t0 : t1) + off + s * 6 * KB + (i < 4 ? (i & 1) : 2) * 2 * KB;
    const v2u a = lds_tr(q), d = lds_tr(q + 256);
    return v4u{a[0], a[1], d[0], d[1]};
  };
  v4u f[2][4], st0, st1;
  float mean[2], rstd[2];
  LAS float* ms = (LAS float*)(c.lds + L_MS + c.w * 128);
  const v4u ones = {0x3f803f80u, 0x3f803f80u, 0x3f803f80u, 0x3f803f80u};
  const rsrc_t rw = __builtin_amdgcn_make_buffer_rsrc((void*)p.wqkv, 0, DM * 3 * DM * 2, 0x00020000);
  const int wvo = (c.lane >> 2) * 3072 + ((c.lane & 3) ^ ((c.lane >> 4) & 2)) * 16;
  const int wS = 8 * (c.w >> 2) + ((c.w >> 1) & 1);
  auto wpiece = [&](int n, int m) __attribute__((always_inline)) NOPK {
    dma1p(rw, c.ldsb + L_A0 + ((wS + 2 * n) * 6 + 2 * m + (c.w & 1)) * KB, wvo, (32 * (wS + 2 * ((n + rot) & 3)) + 16 * (c.w & 1)) * 3072 + m * KB + c.ci * 64);
  };
  const unsigned xs = L_A3 + c.w * 6 * KB;
  const int xv = (c.lane >> 3) * KB + ((c.lane ^ (c.lane >> 3)) & 7) * 16;
  auto xunit = [&](int u) __attribute__((always_inline)) NOPK {
    const int src = xoff + (((u & 1) ^ kh) * 16) * KB + (((u >> 1) + rot) & 3) * 128;
    dma1p(rx, c.ldsb + xs + (u % 3) * 2 * KB, xv, src);
    dma1p(rx, c.ldsb + xs + (u % 3) * 2 * KB + KB, xv, src + 8 * KB);
  };
  const lptr xr0 = c.lds + xs + t * 128 + ((g ^ t) & 7) * 16, xr1 = c.lds + xs + t * 128 + (((4 + g) ^ t) & 7) * 16;
  auto xread = [&](int j) __attribute__((always_inline)) NOPK {
#pragma unroll
    for (int th = 0; th < 2; ++th) {
      act[2 * j][th] = lds_r(xr0 + ((2 * j + th) % 3) * 2 * KB);
      act[2 * j + 1][th] = lds_r(xr1 + ((2 * j + th) % 3) * 2 * KB);
    }
    asm volatile("s_waitcnt lgkmcnt(0)" : "+v"(act[2 * j][0]), "+v"(act[2 * j][1]), "+v"(act[2 * j + 1][0]), "+v"(act[2 * j + 1][1]) :: "memory");
  };
  unsigned tg0 = 0;
  u64 tag = c.tag;
  auto stats0 = [&](v4u a, int voff, float& mu, float& rs) __attribute__((always_inline)) NOPK {
    if (__builtin_expect(!wave_all(a[1] == tg0 && a[3] == tg0), 0)) {
      for (unsigned spins = 0; spins < SPIN_CAP; ++spins) {
        a = ld_sc1(c.rs, voff, W_S0 + TB * 4 * KB);
        asm volatile("" ::: "memory");
        if (wave_all(a[1] == tg0 && a[3] == tg0)) break;
      }
    }
    mu = u2f(a[0]) * (1.f / DM);
    rs = __builtin_amdgcn_rsqf(__builtin_fmaxf(u2f(a[2]) * (1.f / DM) - mu * mu, 0.f) + EPS);
  };
  if (L == 0) {
    const int tok = 128 * c.rb + 8 * c.ci + c.w;
    v4u rr;
    asm volatile("s_nop 4\n\tbuffer_load_dwordx4 %0, %1, %2, %3 offen" : "=&v"(rr) : "v"(c.lane * 16), "s"(rx), "s"(tok * KB) : "memory");
    dma1p(rw, c.ldsb + L_QX + c.w * KB, c.lane * 16, c.w < 6 ? BID * 6 * KB + c.w * KB : 2 * MB);
    xunit(0); xunit(1); xunit(2);
    wpiece(0, 0); wpiece(0, 1); wpiece(0, 2);
    asm volatile("s_waitcnt vmcnt(5)" : "+v"(rr) :: "memory");
    xread(0);
    asm volatile("" : "+s"(epoch));
    tag = (epoch + 1) << 16;
    tg0 = (unsigned)(epoch + 1);
    xunit(3); xunit(4);
    float sm = 0.f, sq = 0.f;
#pragma unroll
    for (int e = 0; e < 4; ++e) { const float a = blo(rr[e]), b = bhi(rr[e]); sm += a + b; sq += a * a + b * b; }
    sm = xhalf_add(xrow_add(dpp_add<0x140>(dpp_add<0x141>(dpp_add<0x4e>(dpp_add<0xb1>(sm))))));
    sq = xhalf_add(xrow_add(dpp_add<0x140>(dpp_add<0x141>(dpp_add<0x4e>(dpp_add<0xb1>(sq))))));
    __builtin_amdgcn_raw_buffer_store_b64(v2u{f2u((c.lane & 1) ? sq : sm), tg0}, c.rs, c.lane < 16 ? c.lane * 8 : W_END, W_S0 + tok * 128, SC1);
    wpiece(1, 0); wpiece(1, 1); wpiece(1, 2);
    WAITV(6);
    xread(1);
    xunit(5); xunit(6);
    tt = tile_task(p, c, BID);
    STAMP(57);
    WAITV(4);
    BAR();
  }
  for (unsigned tries = 0;; ++tries) {
    if (L) {
      if (tries) poll_data(c.rs, x_probe(c.lane), xoff);
      QA(0) SBAR(); QA(1) SBAR(); QA(2)
      SBAR();
      if (tries == 0) { WAITV(6); BAR(); }
    }
    STAMP(20 * L + 2);
    unsigned sn = 0;
#pragma unroll
    for (int i = 0; i < 4; ++i) { f[0][i] = qw(0, P1[i]); acc[P1[i]][0] = f32x4{}; acc[P1[i]][1] = f32x4{}; }
    SBAR();
#pragma unroll
    for (int s = 0; s < 8; ++s) {
      if (L == 0) {
        if (s == 2) { WAITV(8); xread(2); xunit(7); }
        if (s == 3) { WAITV(4); BAR(); }
        if (s == 4) { WAITV(2); xread(3); }
      }
#pragma unroll
      for (int i = 0; i < 4; ++i) {
        const int e = P1[i];
        f[(s + 1) & 1][i] = s + 1 < 8 ? qw(s + 1, e) : qw(i >> 1, 2 * (i & 1));
        if (L && i == 0 && s + 3 < 8) QA(s + 3)
        if (i == 2 && s == (L ? 4 : 2)) {
          st0 = L ? ld_sc1(c.rs, sv0, soff) : ld_sc1(c.rs, (t + 16 * kh) * 128, W_S0 + TB * 4 * KB);
          st1 = L ? ld_sc1(c.rs, sv1, soff) : ld_sc1(c.rs, (t + 16 * (1 - kh)) * 128, W_S0 + TB * 4 * KB);
        }
#pragma unroll
        for (int th = 0; th < 2; ++th)
          acc[e][th] = e < 4 ? mfma16(f[s & 1][i], act[s][th], acc[e][th]) : mfma16(act[s][th], f[s & 1][i], acc[e][th]);
        if (L) {
          if (i == 1 || i == 3) { sn = seen(sn, act[s][i >> 1]); asm volatile("" : "+v"(sn)); }
          if (s == 7 && i == 0) row_stats(c.rs, st0, sv0, soff, mean[0], rstd[0]);
          if (s == 7 && i == 2) row_stats(c.rs, st1, sv1, soff, mean[1], rstd[1]);
        } else {
          if (s < 2 && i > 0) wpiece(2 + s, i - 1);
          const int z = 4 * (s - 3) + i;
          if (z >= 4 && z <= 19 && z % 3 == 1) {
            const int y = (z - 4) / 3;
            *(LAS float*)(c.lds + L_QX + c.w * 384 + (y < 4 ? (y ^ (2 * kh)) : y) * 64 + g * 16 + (t & 3) * 4) = (t & 2) ? ((t & 1) ? cf[3] : cf[2]) : ((t & 1) ? cf[1] : cf[0]);
          }
          if (z >= 2 && z <= 17 && z % 3 == 2) { cf = mfma16(cfa, ones, f32x4{}); cf = mfma16(cfb, ones, cf); }
          if (z >= 0 && z <= 15 && z % 3 == 0) { cfa = qw(0, z / 3, tb * 12 * KB); cfb = qw(1, z / 3, tb * 12 * KB); }
          if (s == 4 && i == 1) res = *(const GAS v4u*)(p.x + (c.rb2 * 64 + c.w * 8 + (c.lane >> 3)) * DM + 64 * c.cb2 + 8 * (c.lane & 7));
          if (s == 4 && i == 3) tile_load(c, tt, raw);
          if (s == 5 && i == 1) stats0(st0, (t + 16 * kh) * 128, mean[0], rstd[0]);
          if (s == 6 && i == 2) stats0(st1, (t + 16 * (1 - kh)) * 128, mean[1], rstd[1]);
        }
        if (s == 7 && i == 3 && g < 2) ms[c.lane] = g ? rstd[0] : mean[0];
        if (s == 1 && i == 3) asm volatile(ATTN_INIT_V : ATTN_OUT_V(ck_.vr) : [one] "v"(c.r == 16 ? 0x3f803f80u : 0u));
        if (s == 2 && i == 3) asm volatile(ATTN_INIT_A : ATTN_OUT_A(ck_));
        if (s == 3 && i == 3) asm volatile(ATTN_INIT_B : ATTN_OUT_B(ck_));
        SBAR();
      }
    }
    if (L == 0 || wave_all(sn != EMPTY) || tries >= 64) break;
  }
#undef QA
#undef QA1
  STAMP(20 * L + 3);
  const lptr ex = c.lds + L_A3 + c.w * 6 * KB + c.lane * 16, exr = c.lds + L_A3 + (c.w ^ 1) * 6 * KB + c.lane * 16;
  const int kvoff = W_KV + L * 4 * MB + (2 * c.ci * 64 + TB) * 2 * KB;
  const int slot = ((g >> 1) * 32 + t + 16 * ((g & 1) ^ kh)) * 16;
  f32x4 mv, rv;
  v4u ek, ev, kf, vf;
#pragma unroll
  for (int i = 0; i < 4; i += 2) { acc[i][0] = f32x4{}; acc[i][1] = f32x4{}; }
#pragma unroll
  for (int n = 0; n < 16; ++n) {
    const int s = n >> 1, e = 2 * (n & 1);
    if (n == 3) {
      BAR();
      if (L == 0) {
        if (c.tid < 96) {
          float sum = 0.f;
#pragma unroll
          for (int j = 0; j < 8; ++j) sum += *(LAS float*)(c.lds + L_QX + j * 384 + c.tid * 4);
          *(LAS float*)(c.lds + L_CS + c.tid * 4) = sum;
        }
      }
      ek = lds_r(exr + KB);
      ev = lds_r(exr + 2 * KB);
    }
    if (L == 0 && n == 5) { BAR(); cs = qkv_colsums(c); }
    if (n + 4 < 16) f[1 - ((n >> 2) & 1)][n & 3] = qw((n + 4) >> 1, e);
    acc[e][0] = mfma16(f[(n >> 2) & 1][n & 3], act[s][0], acc[e][0]);
    acc[e][1] = mfma16(f[(n >> 2) & 1][n & 3], act[s][1], acc[e][1]);
    if (n == 1) {
      lds_w(ex + KB, pk8(acc[3][1], acc[3][0]));
      lds_w(ex + 2 * KB, pk8(acc[4][1], acc[5][1]));
      mv = lds_rf((lptr)ms + g * 16);
      rv = lds_rf((lptr)ms + 64 + g * 16);
    }
    if (n == 5 || n == 6) { acc[1][n - 5] += n == 5 ? up_lo(ek) : up_hi(ek); asm volatile("" : "+v"(acc[1][n - 5])); }
    if (n == 7 || n == 8) { acc[1][n - 7] = (acc[1][n - 7] - mean[n - 7] * cs.k) * rstd[n - 7]; asm volatile("" : "+v"(acc[1][n - 7])); }
    if (n == 9) {
      kf = pair_rows(pk(acc[1][0][0], acc[1][0][1]), pk(acc[1][0][2], acc[1][0][3]), pk(acc[1][1][0], acc[1][1][1]), pk(acc[1][1][2], acc[1][1][3]));
      st_sc1(kf, c.rs, slot, kvoff + kh * 128 * KB);
    }
    if (n == 10 || n == 11) { acc[n - 6][0] += n == 10 ? up_lo(ev) : up_hi(ev); asm volatile("" : "+v"(acc[n - 6][0])); }
    if (n == 12 || n == 13) { acc[n - 8][0] = (acc[n - 8][0] - mv * (n == 12 ? cs.v0 : cs.v1)) * rv; asm volatile("" : "+v"(acc[n - 8][0])); }
    if (n == 14) {
      vf = pair_rows(pk(acc[4][0][0], acc[4][0][1]), pk(acc[4][0][2], acc[4][0][3]), pk(acc[5][0][0], acc[5][0][1]), pk(acc[5][0][2], acc[5][0][3]));
      st_sc1(vf, c.rs, (g & 1) * 128 * KB + (g >> 1) * 256 + t * 16, kvoff + KB + kh * 512);
    }
    SBAR();
  }
  STAMP(20 * L + 4);
  lds_w(ex, pk8(acc[2][1], acc[2][0]));
#pragma unroll
  for (int th = 0; th < 2; ++th) acc[0][th] -= mean[th] * cs.q;
  BAR();
  const v4u eq = lds_r(exr);
  lds_w(c.lds + L_KX + (kh * 4 + tb) * 2 * KB + slot, kf);
  lds_w(c.lds + L_KX + ((g & 1) * 4 + tb) * 2 * KB + KB + kh * 512 + (g >> 1) * 256 + t * 16, vf);
  f32x4 q[2];
#pragma unroll
  for (int th = 0; th < 2; ++th) q[th] = (acc[0][th] + (th ? up_hi(eq) : up_lo(eq))) * (rstd[th] * QSCALE);
  const v4u qf = pair_rows(pk(q[0][0], q[0][1]), pk(q[0][2], q[0][3]), pk(q[1][0], q[1][1]), pk(q[1][2], q[1][3]));
  lds_w(c.lds + L_QX + (kh * 4 + tb) * KB + slot, qf);
  if (L == 0) { tile_put(c, c.lds + L_A1, raw); res = lane_t8(res, c.lane); }
  ck_.qf = qf; ck_.kf = kf; ck_.v[0] = acc[4][0]; ck_.v[1] = acc[5][0];
  DRAIN();
  BAR();
  if (c.tid == 0) post(c.ws, tag, F_Q, L, c.ci, c.rb, 0u);
}

DI v4u wd_load(const Ctx& c, int i) {
  return ld_sc1(c.rs, c.lane * 16 + (i & 3) * KB, W_WD + ((2 * c.cb2 + (c.w & 1)) * 112 + 28 * (c.w >> 1) + (i & ~3)) * KB);
}

DI void attn_safe(const Ctx& c, int L, const v4u (&q)[4], f32x8 (&ov)[4], float (&lv)[4], float (&m)[4], unsigned one) {
  f32x16 o[4];
  const int hh = c.w & 1, kq = c.w >> 1;
  const int hb = W_KV + L * 4 * MB + (2 * c.ci + hh) * 128 * KB;
#pragma unroll
  for (int j = 0; j < 4; ++j) { o[j] = f32x16{}; m[j] = -INFINITY; }
  for (int t = 0; t < 16; ++t) {
    const int so = hb + (4 * t + kq) * 2 * KB;
    v4u kf = ld_sc1(c.rs, c.lane * 16, so);
    v4u v0 = ld_sc1(c.rs, KB + c.h * 256 + (c.r & 15) * 16, so), v1 = ld_sc1(c.rs, KB + 512 + c.h * 256 + (c.r & 15) * 16, so);
    if (c.r >= 16) { v0 = v4u{one, one, one, one}; v1 = v0; }
#pragma unroll
    for (int j = 0; j < 4; ++j) {
      f32x16 s = mfma(kf, q[j], f32x16{});
      float mx = s[0];
#pragma unroll
      for (int i = 1; i < 16; ++i) mx = __builtin_fmaxf(mx, s[i]);
      mx = __builtin_fmaxf(xhalf_max(mx), m[j]);
      const float al = __builtin_amdgcn_exp2f(m[j] - mx);
      m[j] = mx;
      f32x8 p0, p1;
#pragma unroll
      for (int i = 0; i < 8; ++i) { p0[i] = __builtin_amdgcn_exp2f(s[i] - mx); p1[i] = __builtin_amdgcn_exp2f(s[8 + i] - mx); }
#pragma unroll
      for (int i = 0; i < 16; ++i) o[j][i] *= al;
      o[j] = __builtin_amdgcn_smfmac_f32_32x32x32_bf16(__builtin_bit_cast(bf16x8, v0), __builtin_bit_cast(bf16x16, p0), o[j], 0xdddd, 0, 0);
      o[j] = __builtin_amdgcn_smfmac_f32_32x32x32_bf16(__builtin_bit_cast(bf16x8, v1), __builtin_bit_cast(bf16x16, p1), o[j], 0xdddd, 0, 0);
    }
  }
#pragma unroll
  for (int j = 0; j < 4; ++j) {
    lv[j] = o[j][8];
#pragma unroll
    for (int i = 0; i < 8; ++i) ov[j][i] = o[j][i];
  }
}

DI float max3abs(float a, float b, float d) {
  return __builtin_elementwise_maximum(__builtin_elementwise_maximum(__builtin_fabsf(a), __builtin_fabsf(b)), __builtin_fabsf(d));
}
DI float gram_max(v4u f) {
  const f32x16 m = mfma(f, f, f32x16{});
  float x = max3abs(m[0], m[1], m[2]);
#pragma unroll
  for (int i = 3; i < 15; i += 2) x = max3abs(x, m[i], m[i + 1]);
  return max3abs(x, m[15], m[15]);
}
DI void phase_attn(const Ctx& c, int L, v4u (&wd)[28], const Tile& tt, Chk& ck) {
  STAMP(20 * L + 5);
  const int hh = c.w & 1, kq = c.w >> 1;
  v4u q[4];
#pragma unroll
  for (int j = 0; j < 4; ++j) q[j] = lds_r(c.lds + L_QX + (hh * 4 + (kq ^ j)) * KB + c.lane * 16);
  const u64 vmask = 0x0000FFFF0000FFFFull;
  f32x8 o[4] = {ck.o[0], ck.o[1], ck.o[2], ck.o[3]}, vr[2] = {ck.vr[0], ck.vr[1]};
  float lq[4] = {ck.l0, ck.l1, ck.l2, ck.l3};
  const unsigned lk = c.ldsb + L_KX + (hh * 4 + kq) * 2 * KB;
  asm volatile(ATTN_OWN_ASM
               : ATTN_STATE(o, lq, vr)
               : ATTN_Q(q), [vm] "s"(vmask), [lk] "v"(lk + c.lane * 16), [lv] "v"(lk + KB + c.h * 256 + (c.r & 15) * 16)
               : ATTN_CLOB, "memory");
  STAMP(40 * L + 20);
  const float v0 = max3abs(max3abs(ck.v[0][0], ck.v[0][1], ck.v[0][2]), ck.v[0][3], 0.f), v1 = max3abs(max3abs(ck.v[1][0], ck.v[1][1], ck.v[1][2]), ck.v[1][3], 0.f);
  const bool ok = wave_all(gram_max(ck.qf) <= 22.8f && gram_max(ck.kf) <= 175.f && v0 <= 1e12f && v1 <= 1e12f);
  const unsigned bits = (ok ? 0u : 1u) | (wave_all(v0 < 1e-12f) ? 0u : 2u) | (wave_all(v1 < 1e-12f) ? 0u : 4u);
  if (c.lane == 0) *(LAS unsigned*)(c.lds + L_NR + 16 + c.w * 4) = bits;
  unsigned csum = 0;
  if (L == 0) {
    v4u fr[4];
    tile_read(c, tt, c.lds + L_A1, fr);
#pragma unroll
    for (int e = 0; e < 4; ++e) wd[e] = fr[e];
    csum = tile_colsum(c, wd);
  }
  poll16(c.ws, c.tag, F_Q, L, c.ci, 0u, c.w, c.lane);
  if (L == 0) {
    __builtin_amdgcn_raw_buffer_store_b32(csum, c.rs, c.lane < 32 ? c.lane * 4 : W_END, tt.dcs, SC1);
    st_sc1(v4u{EMPTY, EMPTY, EMPTY, EMPTY}, c.rs, c.tid < 96 ? c.tid * 16 : W_END, W_ST + ST_BYTES + BID * 1536);
  }
  BAR();
  STAMP(20 * L + 6);
  if (c.tid == 0) {
    const v4u n0 = lds_r(c.lds + L_NR + 16), n1 = lds_r(c.lds + L_NR + 32);
    post(c.ws, c.tag, F_C, L, c.ci, c.rb, n0[0] | n0[1] | n0[2] | n0[3] | n1[0] | n1[1] | n1[2] | n1[3]);
  }
  {
    unsigned pm = c.ldsb + L_WO + c.w * 8 * KB, ps = W_WO + c.cb2 * 64 * KB + c.w * 8 * KB;
    unsigned vok = c.lane * 16, vov = KB + c.h * 256 + (c.r & 15) * 16;
    unsigned hb = W_KV + L * 4 * MB + (2 * c.ci + hh) * 128 * KB, ti = ((c.rb * 4 + kq + 4) & 63) * 2 * KB, so = hb + ti, st = 8 * KB, n = ATTN_NITER;
    unsigned pw = W_WD + ((2 * c.cb2 + (c.w & 1)) * 112 + 28 * (c.w >> 1)) * KB;
    unsigned gc = L ? 0u : SPIN_CAP, gs = W_START;
    unsigned fx = xfrag_soff(c, 1), fo = of_soff(c, 0);
    u64 tmp;
    asm volatile(ATTN_LOOP_ASM
                 : ATTN_STATE(o, lq, vr), ATTN_WD(wd), [ti] "+s"(ti), [so] "+s"(so), [n] "+s"(n), [pm] "+s"(pm), [ps] "+s"(ps), [pw] "+s"(pw),
                   [gc] "+s"(gc), [tmp] "=&s"(tmp), [fx] "+s"(fx), [fo] "+s"(fo)
                 : ATTN_Q(q), "{v4}"(vok), "{v5}"(vov), [rs] "s"(c.rs), [vm] "s"(vmask), [st] "s"(st), [hb] "s"(hb), [gs] "s"(gs),
                   [tg] "s"(c.tag), [tl] "s"((unsigned)c.tag), [th] "s"((unsigned)(c.tag >> 32)), [vx] "v"(xfrag_voff(c)), [wv] "s"(c.w),
                   [sp] "s"(W_START + BID * 8), [td0] "s"(tile_dst(tt, 0)), [td1] "s"(tile_dst(tt, 1)), [td2] "s"(tile_dst(tt, 2)), [td3] "s"(tile_dst(tt, 3))
                 : ATTN_CLOB, "vcc", "scc", "memory");
  }
  STAMP(20 * L + 7);
  float m[4] = {0.f, 0.f, 0.f, 0.f};
  auto put = [&]() __attribute__((always_inline)) NOPK {
#pragma unroll
    for (int j = 1; j < 4; ++j) {
      lptr ax = c.lds + L_A0 + ((c.w ^ (2 * j)) * 3 + (j - 1)) * 2560;
      lds_wf(ax + c.lane * 16, f32x4{o[j][0], o[j][1], o[j][2], o[j][3]});
      lds_wf(ax + KB + c.lane * 16, f32x4{o[j][4], o[j][5], o[j][6], o[j][7]});
      *(LAS f32x2*)(ax + 2 * KB + c.lane * 8) = f32x2{lq[j], m[j]};
    }
  };
  put();
  if (c.w < 4) {
    float cp[8];
#pragma unroll
    for (int j = 0; j < 8; ++j) cp[j] = u2f(__builtin_amdgcn_raw_buffer_load_b32(c.rs, c.tid * 4, W_CSG + j * CSG_BYTES + c.ci * 896, SC1));
    if (c.tid < 224) *(LAS float*)(c.lds + L_CS + 384 + c.tid * 4) = ((cp[0] + cp[1]) + (cp[2] + cp[3])) + ((cp[4] + cp[5]) + (cp[6] + cp[7]));
  }
  if (c.w == 0) {
    GAS u64* f = flag_word(c.ws, F_C, L, c.ci, c.lane & 15);
    u64 g = 0;
    for (unsigned spins = 0; spins < SPIN_CAP; ++spins) {
      g = __hip_atomic_load(f, RLX_AGENT);
      if (wave_all(flag_ge(g, c.tag, 0u))) break;
    }
    const bool in_range = wave_all(((unsigned)g & 1u) == 0) && !wave_all(((unsigned)g & 2u) == 0) && !wave_all(((unsigned)g & 4u) == 0);
    if (c.lane == 0) *(LAS unsigned*)(c.lds + L_DEC) = in_range ? 1u : 0u;
  }
  DRAIN();
  BAR();
  STAMP(20 * L + 8);
  const bool fast = __builtin_amdgcn_readfirstlane(*(LAS unsigned*)(c.lds + L_DEC)) != 0;
  if (!fast) {
    attn_safe(c, L, q, o, lq, m, c.r == 16 ? 0x3f803f80u : 0u);
    put();
    BAR();
  }
  float ov[8], l = lq[0];
#pragma unroll
  for (int i = 0; i < 8; ++i) ov[i] = o[0][i];
  if (fast) {
#pragma unroll
    for (int j = 0; j < 3; ++j) {
      lptr ax = c.lds + L_A0 + (c.w * 3 + j) * 2560;
      f32x4 a = lds_rf(ax + c.lane * 16), b = lds_rf(ax + KB + c.lane * 16);
      l += *(LAS float*)(ax + 2 * KB + c.lane * 8);
#pragma unroll
      for (int e = 0; e < 4; ++e) { ov[e] += a[e]; ov[4 + e] += b[e]; }
    }
  } else {
    float mj[3], mx = m[0];
#pragma unroll
    for (int j = 0; j < 3; ++j) { mj[j] = *(LAS float*)(c.lds + L_A0 + (c.w * 3 + j) * 2560 + 2 * KB + c.lane * 8 + 4); mx = __builtin_fmaxf(mx, mj[j]); }
    const float a0 = __builtin_amdgcn_exp2f(m[0] - mx);
    l *= a0;
#pragma unroll
    for (int i = 0; i < 8; ++i) ov[i] *= a0;
#pragma unroll
    for (int j = 0; j < 3; ++j) {
      lptr ax = c.lds + L_A0 + (c.w * 3 + j) * 2560;
      f32x4 a = lds_rf(ax + c.lane * 16), b = lds_rf(ax + KB + c.lane * 16);
      const float aj = __builtin_amdgcn_exp2f(mj[j] - mx);
      l += aj * *(LAS float*)(ax + 2 * KB + c.lane * 8);
#pragma unroll
      for (int e = 0; e < 4; ++e) { ov[e] += aj * a[e]; ov[4 + e] += aj * b[e]; }
    }
  }
  const float il = 1.f / xhalf_add(l);
  st_sc1(no_empty(v4u{pk(ov[0] * il, ov[1] * il), pk(ov[2] * il, ov[3] * il), pk(ov[4] * il, ov[5] * il), pk(ov[6] * il, ov[7] * il)}), c.rs,
         c.lane * 16, of_soff(c, L));
}

DI void red_put(lptr base, int slot, const Ctx& c, const f32x16& a, int i0) {
  auto x = __builtin_amdgcn_permlane32_swap(pk(a[i0], a[i0 + 1]), pk(a[i0 + 4], a[i0 + 5]), false, false);
  auto y = __builtin_amdgcn_permlane32_swap(pk(a[i0 + 2], a[i0 + 3]), pk(a[i0 + 6], a[i0 + 7]), false, false);
  lds_w(base + slot * KB + (c.lane ^ (c.h * 8)) * 16, v4u{x[0], y[0], x[1], y[1]});
}
DI v4u red_get(lptr base, const Ctx& c, v4u res, int xi) {
  const int t = c.lane & 7, ch = c.lane >> 3;
  lptr p = base + ((c.w >> 2) * 4 + (ch >> 1)) * 4 * KB + (ch & 1) * 512 + (((c.w & 3) ^ (ch & 1)) * 8 + t) * 16;
  v4u pa[4];
#pragma unroll
  for (int j = 0; j < 4; ++j) pa[j] = lds_r(p + j * KB);
  f32x4 a = up_lo(res), b = up_hi(res);
#pragma unroll
  for (int j = 0; j < 4; ++j) { a += up_lo(pa[j]); b += up_hi(pa[j]); }
  v4u xf = {pk(a[0], a[1]), pk(a[2], a[3]), pk(b[0], b[1]), pk(b[2], b[3])};
  if (xi >= 0) {
    xf = no_empty(xf);
    st_sc1(xf, c.rs, xfrag_voff(c), xfrag_soff(c, xi));
    float s = 0.f, ss = 0.f;
#pragma unroll
    for (int e = 0; e < 4; ++e) { s += a[e] + b[e]; ss += a[e] * a[e]; ss += b[e] * b[e]; }
    s = xhalf_add(xrow_add(dpp_add<0x128>(s)));
    ss = xhalf_add(xrow_add(dpp_add<0x128>(ss)));
    __builtin_amdgcn_raw_buffer_store_b64(v2u{umin(f2u(s), EMPTY - 1), umin(f2u(ss), EMPTY - 1)}, c.rs, st_voff(c), st_soff(c, xi), SC1);
  }
  return xf;
}

DI v4u phase_oproj(const Ctx& c, int L, v4u res) {
  STAMP(20 * L + 9);
  const int tb = c.w >> 2, kq = c.w & 3, TB = 2 * c.rb2 + tb;
  lptr wp = c.lds + L_WO + 8 * kq * KB + c.lane * 16;
  v4u f[16];
#pragma unroll
  for (int s = 0; s < 8; ++s) { f[2 * s] = lds_r(wp + s * KB); f[2 * s + 1] = lds_r(wp + (32 + s) * KB); }
  BAR();
  const unsigned gdst = c.ldsb + c.w * 4 * KB, gsrc = W_WG + 7 * c.ci * 32 * KB + c.w * 4 * KB;
  dma4<true>(c.rs, gdst, c.lane * 16, gsrc);
  dma4<true>(c.rs, gdst + 32 * KB, c.lane * 16, gsrc + 32 * KB);
  const int ooff = W_OF + L * 2 * MB + (TB * 32 + 8 * kq) * KB;
  v4u act[8];
  f32x16 a0, a1;
#define OA(s) act[s] = ld_sc1(c.rs, c.lane * 16 + ((s) & 3) * KB, ooff + ((s) & 4) * KB);
  for (unsigned tries = 0;; ++tries) {
    if (tries) poll_data(c.rs, (c.lane & 7) * KB + 1020, ooff);
    STAMP(20 * L + 10);
    OA(0) SBAR(); OA(1) SBAR(); OA(2) SBAR(); OA(3)
    SBAR();
    a0 = f32x16{};
    a1 = f32x16{};
    unsigned sn = 0;
#pragma unroll
    for (int s = 0; s < 8; ++s) {
      if (s + 4 < 8) OA(s + 4)
      a0 = mfma(f[2 * s], act[s], a0);
      sn = seen(sn, act[s]);
      asm volatile("" : "+v"(sn));
      a1 = mfma(f[2 * s + 1], act[s], a1);
      SBAR();
    }
    if (wave_all(sn != EMPTY) || tries >= 64) break;
  }
#undef OA
  STAMP(20 * L + 11);
  lptr rx = c.lds + L_WO;
  red_put(rx, (tb * 4 + 0) * 4 + kq, c, a0, 0);
  red_put(rx, (tb * 4 + 1) * 4 + kq, c, a0, 8);
  red_put(rx, (tb * 4 + 2) * 4 + kq, c, a1, 0);
  red_put(rx, (tb * 4 + 3) * 4 + kq, c, a1, 8);
  BAR();
  const v4u xf = red_get(rx, c, res, 2 * L + 1);
  BAR();
  return xf;
}

#define H_SLOT(u) ((u) < 5 ? (u) : 7 - (u))
template <int OFF> DI void dma1s(rsrc_t rs, unsigned lds, unsigned voff, unsigned soff) {
  asm volatile("s_mov_b32 m0, %0\n\ts_nop 0\n\tbuffer_load_dwordx4 %1, %2, %3 offen offset:%4 sc1 lds" :: "s"(lds), "v"(voff), "s"(rs), "s"(soff), "i"(OFF) : "memory");
}

DI void phase_gu(const Ctx& c, int L, v4u (&wd)[28]) {
  STAMP(20 * L + 12);
  const int tb = c.w & 3, kh = c.w >> 2, TB = 4 * c.rb + tb, t = c.lane & 15, g = c.lane >> 4;
  const int wsrc = W_WG + (7 * c.ci) * 32 * KB + c.w * 4 * KB;
  const unsigned wdst = c.ldsb + c.w * 4 * KB, l16 = c.lane * 16;
#pragma unroll
  for (int u = 2; u < 4; ++u) dma4<true>(c.rs, wdst + u * 32 * KB, l16, wsrc + u * 32 * KB);
  const int xoff = W_X + (2 * L + 1) * 2 * MB + (TB * 32 + 16 * kh) * KB;

  const int av = (g >> 1) * KB + ((g & 1) * 32 + t) * 16, av0 = av + kh * 256, av1 = av + (1 - kh) * 256;
  v4u act[8][2];
#define GA(s) { act[s][0] = ld_sc1(c.rs, av0 + ((s) & 1) * 2 * KB, xoff + ((s) >> 1) * 4 * KB); act[s][1] = ld_sc1(c.rs, av1 + ((s) & 1) * 2 * KB, xoff + ((s) >> 1) * 4 * KB); }
  const lptr wb0 = c.lds + kh * 16 * KB + c.lane * 16, wb1 = opaque(wb0 + 64 * KB);
#define GW(u, i) lds_r((((u) & 2) ? wb1 : wb0) + ((u) & 1) * 32 * KB + (i) * KB)
  constexpr int D = 6;
  v4u f[8];
  v4u st;
  const int soff = W_ST + (2 * L + 1) * ST_BYTES + TB * 32 * 64, sv = (t + 16 * kh) * 16 + g * 512;
  float mean = 0.f, r2 = 0.f;
  f32x4 p[2][2], og, ou, cg, cu;
  unsigned y0 = 0, y1 = 0;
  const int hoff = W_HF + L * 8 * MB + (TB * 112 + 28 * (c.ci >> 2) + (c.ci & 3)) * KB;
  const int hv = ((g >> 1) * 32 + t + 16 * kh) * 16 + (g & 1) * 8;
  const lptr ex0 = opaque(c.lds + L_GX + tb * 4 * KB + kh * 2 * KB + c.lane * 16), ex1 = opaque(c.lds + L_GX + tb * 4 * KB + (1 - kh) * 2 * KB + c.lane * 16);
  const lptr csb = opaque(c.lds + L_CS + 3 * 128 + g * 16);
  auto fin = [&](int u, int k) __attribute__((always_inline)) NOPK {
    const lptr ew = (u & 1) ? ex1 : ex0, er = (u & 1) ? ex0 : ex1;
    if (k == 2) lds_wf(ew, p[0][1]);
    if (k == 4) cg = lds_rf(csb + u * 128);
    if (k == 6) lds_wf(ew + KB, p[1][1]);
    if (k == 8) cu = lds_rf(csb + u * 128 + 64);
    if (k >= 10 && k < 14) { p[0][0][k - 10] -= mean * cg[k - 10]; asm volatile("" : "+v"(p[0][0])); }
    if (k == 16) og = lds_rf(er);
    if (k == 17) ou = lds_rf(er + KB);
    if (k >= 14 && k < 18) { p[1][0][k - 14] -= mean * cu[k - 14]; asm volatile("" : "+v"(p[1][0])); }
    if (k >= 22 && k < 26) { p[0][0][k - 22] += og[k - 22]; p[1][0][k - 22] += ou[k - 22]; asm volatile("" : "+v"(p[0][0]), "+v"(p[1][0])); }
    if (k >= 26 && k < 30) { p[0][0][k - 26] *= p[1][0][k - 26] * r2; asm volatile("" : "+v"(p[0][0])); }
    if (k == 30) { y0 = pk(p[0][0][0], p[0][0][1]); y1 = pk(p[0][0][2], p[0][0][3]); asm volatile("" : "+v"(y0), "+v"(y1)); }
    if (k == 31) __builtin_amdgcn_raw_buffer_store_b64(v2u{y0, y1}, c.rs, hv, hoff + u * 4 * KB, SC1);
  };
  u64 pv = 0;
  unsigned pa;
  const int pf = flag_off(F_G, L, c.ro);
  for (unsigned tries = 0;; ++tries) {
    if (tries) poll_data(c.rs, x_probe(c.lane), xoff);
    STAMP(20 * L + 13);
    GA(0) SBAR(); GA(1) SBAR(); GA(2) SBAR(); GA(3)
#pragma unroll
    for (int i = 0; i < D; ++i) f[i] = GW(0, i);
    SBAR();
    unsigned sn = 0;
#pragma unroll
    for (int m = 0; m < 2; ++m) { p[m][0] = f32x4{}; p[m][1] = f32x4{}; }
#pragma unroll
    for (int i = 0; i < 16; ++i) {
      f[(i + D) & 7] = i + D < 16 ? GW(0, i + D) : GW(1, i + D - 16);
      if ((i & 1) == 0 && i < 8) GA(4 + (i >> 1))
      if (i == 8) st = ld_sc1(c.rs, sv, soff);
      p[i & 1][0] = mfma16(f[i & 7], act[i >> 1][0], p[i & 1][0]);
      sn = seen(sn, act[i >> 1][i & 1]);
      asm volatile("" : "+v"(sn));
      p[i & 1][1] = mfma16(f[i & 7], act[i >> 1][1], p[i & 1][1]);
      SBAR();
    }
    if (wave_all(sn != EMPTY) || tries >= 64) break;
  }
  STAMP(20 * L + 14);
#pragma unroll
  for (int u = 1; u < 7; ++u) {
    f32x4 a[2][2] = {};
#pragma unroll
    for (int i = 0; i < 16; ++i) {
      if (i == 8) {
        if (u == 3 || u == 4) { WAITV(5); } else if (u == 5) { WAITV(1); } else if (u == 6) { WAITV(3); }
        asm volatile("s_waitcnt lgkmcnt(4)\n\ts_barrier" ::: "memory");
        if (u == 6 && c.tid == 0) post(c.ws, c.tag, F_G, L, c.rb, c.ci, 4u);
        if (L == 0) STAMP2(50 + u);
      }
      if (u < 6 || i + D < 16) f[(i + D) & 7] = i + D < 16 ? GW(u, i + D) : GW(u + 1, i + D - 16);
      a[i & 1][0] = mfma16(f[i & 7], act[i >> 1][0], a[i & 1][0]);
      if (u == 6 && i == 15 && c.w == 0) asm volatile("v_lshrrev_b32 %0, 1, %2\n\tv_and_b32 %0, 0x78, %0\n\tbuffer_load_dwordx2 %1, %0, %3, %4 offen sc1" : "=&v"(pa), "=&v"(pv) : "v"(l16), "s"(c.rs), "s"(pf) : "memory");
      if (u > 0) fin(u - 1, 2 * i);
      if (u == 1 && i == 0) { float rstd; row_stats(c.rs, st, sv, soff, mean, rstd); r2 = rstd * rstd; }
      SBAR();
      a[i & 1][1] = mfma16(f[i & 7], act[i >> 1][1], a[i & 1][1]);
      if (u > 0) fin(u - 1, 2 * i + 1);
      if (u >= 1 && u <= 3) {
        if (i == 9) dma1<0>(c.rs, wdst + ((u + 3) & 3) * 32 * KB, l16, wsrc + (u + 3) * 32 * KB);
        if (i == 13) dma1<1024>(c.rs, wdst + ((u + 3) & 3) * 32 * KB, l16, wsrc + (u + 3) * 32 * KB);
      }
      if (u >= 2 && u <= 4) {
        if (i == 1) dma1<2048>(c.rs, wdst + ((u + 2) & 3) * 32 * KB, l16, wsrc + (u + 2) * 32 * KB);
        if (i == 5) dma1<3072>(c.rs, wdst + ((u + 2) & 3) * 32 * KB, l16, wsrc + (u + 2) * 32 * KB);
      }
      if (u == 6 && (i & 3) == 1) wd[24 + (i >> 2)] = wd_load(c, 24 + (i >> 2));
      SBAR();
    }
#pragma unroll
    for (int m = 0; m < 2; ++m) { p[m][0] = a[m][0]; p[m][1] = a[m][1]; }
  }
  STAMP(20 * L + 15);
#pragma unroll
  for (int k = 0; k < 16; ++k) fin(6, k);
  PIN4(wd, 24);
  if (c.w == 0) {
    asm volatile("s_waitcnt vmcnt(1)" : "+v"(pv) :: "memory");
    if (!wave_all(flag_ge(pv, c.tag, 4u))) poll16(c.ws, c.tag, F_G, L, c.ro, 4u, 0, c.lane);
  }
  BAR();
#pragma unroll
  for (int k = 16; k < 32; ++k) fin(6, k);
#undef GA
#undef GW
}

DI v4u phase_down(const Ctx& c, int L, v4u res, v4u (&wd)[28], unsigned short* out, Cs& cs) {
  STAMP(20 * L + 16);
  const int nb = c.w & 1, kq = c.w >> 1;
  const lptr hp0 = c.lds + kq * 8 * KB + c.lane * 16, hp1 = opaque(hp0 + 64 * KB), hp2 = opaque(hp0 + 128 * KB);
#define DH(u, i) lds_r((H_SLOT(u) == 4 ? hp2 : (H_SLOT(u) & 2) ? hp1 : hp0) + (H_SLOT(u) & 1) * 32 * KB + (i) * KB)
  cs = qkv_colsums(c);
  const int hsrc = W_HF + L * 8 * MB + ((2 * c.rb2 + nb) * 112 + 28 * kq) * KB;
  const unsigned hdst = c.ldsb + c.w * 4 * KB, l16 = c.lane * 16;
#define HS(u) dma4<true>(c.rs, hdst + H_SLOT(u) * 32 * KB, l16, hsrc + (u) * 4 * KB);
#define HP(u, jj) { if ((jj) == 0) dma1s<0>(c.rs, hdst + H_SLOT(u) * 32 * KB, l16, hsrc + (u) * 4 * KB); \
                    if ((jj) == 1) dma1s<1024>(c.rs, hdst + H_SLOT(u) * 32 * KB, l16, hsrc + (u) * 4 * KB); \
                    if ((jj) == 2) dma1s<2048>(c.rs, hdst + H_SLOT(u) * 32 * KB, l16, hsrc + (u) * 4 * KB); \
                    if ((jj) == 3) dma1s<3072>(c.rs, hdst + H_SLOT(u) * 32 * KB, l16, hsrc + (u) * 4 * KB); }
  const int qsrc = W_WQ + c.ci * 96 * KB + c.w * 4 * KB;
  HS(0) HS(1)
  WAITV(4);
  BAR();
  if (c.tid == 0) post(c.ws, c.tag, F_G, L, c.rb, c.ci, 7u);
  STAMP(20 * L + 17);
  v4u f[2][8];
#pragma unroll
  for (int i = 0; i < 8; ++i) f[0][i] = DH(0, i);
  HS(2)
  f32x16 a0 = {}, a1 = {};
#pragma unroll
  for (int u = 0; u < 7; ++u) {
    if (u == 1) poll16(c.ws, c.tag, F_G, L, c.ro, 7u, c.w, c.lane);
    if (u < 5) WAITV(4);
    if (u == 5) { if (L == 0) { WAITV(4); } else { DRAIN(); } }
    if (u < 6) BAR();
    if (u > 0 && u < 6 && L == 0) STAMP2(40 + u);
#pragma unroll
    for (int jj = 0; jj < 4; ++jj) {
      if (u < 6) { f[(u + 1) & 1][jj] = DH(u + 1, jj); f[(u + 1) & 1][4 + jj] = DH(u + 1, 4 + jj); }
      a0 = mfma(wd[4 * u + jj], f[u & 1][jj], a0);
      a1 = mfma(wd[4 * u + jj], f[u & 1][4 + jj], a1);
      if (u < 4) HP(u + 3, jj)
      if (u == 4 && L == 0) { if (jj == 0) dma1<0>(c.rs, hdst, l16, qsrc); if (jj == 1) dma1<1024>(c.rs, hdst, l16, qsrc);
                              if (jj == 2) dma1<2048>(c.rs, hdst, l16, qsrc); if (jj == 3) dma1<3072>(c.rs, hdst, l16, qsrc); }
      if (u == 5 && L == 0) { if (jj == 0) dma1<0>(c.rs, hdst + 64 * KB, l16, qsrc + 64 * KB); if (jj == 1) dma1<1024>(c.rs, hdst + 64 * KB, l16, qsrc + 64 * KB);
                              if (jj == 2) dma1<2048>(c.rs, hdst + 64 * KB, l16, qsrc + 64 * KB); if (jj == 3) dma1<3072>(c.rs, hdst + 64 * KB, l16, qsrc + 64 * KB); }
      SBAR();
    }
  }
#undef HS
#undef HP
#undef DH
  STAMP(20 * L + 18);
  lptr rx = c.lds + L_A3;
  red_put(rx, (0 + 2 * nb) * 4 + kq, c, a0, 0);
  red_put(rx, (1 + 2 * nb) * 4 + kq, c, a0, 8);
  red_put(rx, (4 + 2 * nb) * 4 + kq, c, a1, 0);
  red_put(rx, (5 + 2 * nb) * 4 + kq, c, a1, 8);
  if (L == 0) STAMP2(46);
  BAR();
  if (L == 0) STAMP2(47);
  const v4u xf = red_get(rx, c, res, L ? -1 : 2);
  if (L == 0) dma4<true>(c.rs, hdst + 32 * KB, l16, qsrc + 32 * KB);
  if (L) *(GAS v4u*)(out + (c.rb2 * 64 + c.w * 8 + (c.lane >> 3)) * DM + 64 * c.cb2 + 8 * (c.lane & 7)) = lane_t8(xf, c.lane);
  if (L == 0) STAMP(19);
  return xf;
}

__global__ NOPK void __launch_bounds__(NTHR, 2) k_main(Params p) {
  asm volatile("" :: "s"(p.x), "s"(p.wqkv), "s"(p.wo), "s"(p.wg), "s"(p.wu), "s"(p.wd), "s"(p.out), "s"(p.ws));
  extern __shared__ __attribute__((aligned(16))) unsigned char smem[];
  Ctx c;
  c.ws = p.ws;
  c.rs = __builtin_amdgcn_make_buffer_rsrc(p.ws, 0, W_END, 0x00020000);
  c.lds = (lptr)smem;
  c.ldsb = (unsigned)(uintptr_t)c.lds;
  const int b = BID, xcd = b & 7, ix = b >> 3;
  c.rb = (xcd >> 1) * 4 + (ix >> 3);
  c.ci = (xcd & 1) * 8 + (ix & 7);
  c.ro = 2 * (xcd ^ 4) + (ix >> 4);
  c.io = ix & 15;
  c.cb2 = c.io & 7;
  c.rb2 = 2 * c.ro + (c.io >> 3);
  c.w = __builtin_amdgcn_readfirstlane(TID >> 6);
  c.lane = TID & 63;
  c.r = c.lane & 31;
  c.h = c.lane >> 5;
  c.tid = TID;
  STAMP(0);
  u64 epoch;
  asm volatile("s_load_dwordx2 %0, %1, %2" : "=s"(epoch) : "s"(p.ws), "i"(W_EPOCH) : "memory");
  early_reads(p, c);
  v4u res, raw[4];
  Tile tt;
  Chk ck;
  STAMP(1);
  c.tag = 0;
  phase_qkv<0>(p, fresh(c), res, raw, tt, ck, Cs{}, epoch);
  c.tag = (epoch + 1) << 16;
  for (int L = 0; L < 2; ++L) {
    v4u wd[28];
#pragma unroll
    for (int e = 0; e < 4; ++e) asm volatile("" : "=v"(wd[e]));
    phase_attn(fresh(c), L, wd, tt, ck);
    res = phase_oproj(fresh(c), L, res);
    phase_gu(fresh(c), L, wd);
    Cs cs;
    res = phase_down(fresh(c), L, res, wd, p.out, cs);
    asm volatile("" : "=s"(tt.d0));
    asm volatile("" : "=s"(tt.d1));
    asm volatile("" : "=s"(tt.d2));
    asm volatile("" : "=s"(tt.dcs));
    asm volatile("" : "=s"(tt.kind));
    asm volatile("" : "=v"(ck.qf), "=v"(ck.kf), "=v"(ck.v[0]), "=v"(ck.v[1]));
    asm volatile("" : ATTN_OUT_V(ck.vr), ATTN_OUT_A(ck), ATTN_OUT_B(ck));
    if (L == 0) {
      STAMP(21);
      phase_qkv<1>(p, fresh(c), res, raw, tt, ck, cs, epoch);
    }
  }
  if (BID == 0 && TID == 0) *(GAS u64*)(p.ws + W_EPOCH) = epoch + 1;
  STAMP(40);
}

}

extern "C" void kernel_launch(void* const* d_in, const int* in_sizes, int n_in, void* d_out, int out_size, void* d_ws, size_t ws_size,
                              hipStream_t stream) {
  Params p;
  p.x = (const unsigned short*)d_in[0];
  p.wqkv = (const unsigned short*)d_in[1];
  p.wo = (const unsigned short*)d_in[2];
  p.wg = (const unsigned short*)d_in[4];
  p.wu = (const unsigned short*)d_in[5];
  p.wd = (const unsigned short*)d_in[6];
  p.out = (unsigned short*)d_out;
  p.ws = (unsigned char*)d_ws;
  (void)hipFuncSetAttribute((const void*)k_main, hipFuncAttributeMaxDynamicSharedMemorySize, LDS_BYTES);
  k_main<<<NWG, NTHR, LDS_BYTES, stream>>>(p);
}
```
